```python
import torch

def _softmax(x, axis=-1):
    return torch.softmax(x, dim=axis)
def _gelu(x):
    return torch.nn.functional.gelu(x, approximate="tanh")
def _silu(x):
    return torch.nn.functional.silu(x)
def _sigmoid(x):
    return torch.sigmoid(x)
def _layernorm(x, axis=-1, eps=1e-5):
    m = x.mean(dim=axis, keepdim=True)
    v = x.var(dim=axis, keepdim=True, unbiased=False)
    return (x - m) / torch.sqrt(v + eps)
def _scatter_add(tgt, idx, src, axis=0):
    return tgt.clone().index_add_(axis, idx, src)
def _segment_sum(src, seg, n):
    out = torch.zeros((n,) + src.shape[1:], dtype=src.dtype)
    return out.index_add_(0, seg, src)
def _pad(x, pw):
    flat = tuple(v for pair in reversed(pw) for v in pair)
    return torch.nn.functional.pad(x, flat)

kernel_name = 'standalone__edge_featured_mpnn_egc__graph'

def setup_inputs():

    _w = (lambda a: (lambda t: t.to(torch.bfloat16) if t.is_floating_point() else t)(torch.as_tensor(a)))
    N, deg, D, De = 25000, 32, 64, max(4, 1)
    E = N * deg
    return {
        "x": _w((lambda *s: torch.randn(*s, dtype=torch.bfloat16) * 0.2)(N, D)),
        "e_attr": _w((lambda *s: torch.randn(*s, dtype=torch.bfloat16) * 0.2)(E, De)),
        "src": _w((lambda lo, hi, *s: torch.randint(lo, hi, s))(0, N, E).to(torch.int64)),
        "dst": _w((lambda lo, hi, *s: torch.randint(lo, hi, s))(0, N, E).to(torch.int64)),
        "u_w": _w((lambda *s: torch.randn(*s, dtype=torch.bfloat16) * 0.05)(D, D)),
        "v_w": _w((lambda *s: torch.randn(*s, dtype=torch.bfloat16) * 0.05)(D, D)),
        "g_w": _w((lambda *s: torch.randn(*s, dtype=torch.bfloat16) * 0.2)(2 * D + De, D)),
    }

def reference(x, e_attr, src, dst, u_w, v_w, g_w):
    N, D = x.shape
    g_in = torch.concatenate([x[dst], x[src], e_attr], axis=-1)
    eta = _sigmoid(g_in @ g_w)
    hv = x @ v_w
    msg = eta * hv[src]
    agg = _segment_sum(msg, dst, N)
    return _silu(x @ u_w + agg)
```

```cpp
#include <hip/hip_runtime.h>
#include <hip/hip_bf16.h>
#include <stdint.h>

typedef __attribute__((ext_vector_type(4))) unsigned u32x4;
typedef __attribute__((ext_vector_type(3))) unsigned u32x3;
typedef __attribute__((ext_vector_type(2))) unsigned u32x2;
typedef __attribute__((ext_vector_type(8))) short bf16x8;
typedef __attribute__((ext_vector_type(4))) short bf16x4;
typedef __attribute__((ext_vector_type(16))) float f32x16;
typedef __attribute__((ext_vector_type(4))) float f32x4;
typedef __attribute__((ext_vector_type(2))) float f32x2;

#define DI __device__ __forceinline__

constexpr int NN = 25000;
constexpr int NE = 800000;
constexpr int NB = 256;
constexpr int NPB = 98;
constexpr int QPW = 782;
constexpr int CHUNK = QPW * 4;
constexpr int NQUAD = NE / 4;

constexpr size_t WS_REC = 0;
constexpr size_t WS_TAB = (size_t)NB * CHUNK * 12;
constexpr size_t WS_WF = WS_TAB + (size_t)NB * NB * 4;
constexpr int WF_C = 4 * 8192;

constexpr float NLOG2E = -1.4426950408889634f;

DI float bf2f(unsigned short v) { return __uint_as_float((unsigned)v << 16); }
DI unsigned short f2bf(float f) { __hip_bfloat16 b = __float2bfloat16(f); return *(unsigned short*)&b; }
DI unsigned pack_bf2(float lo, float hi) { return (unsigned)f2bf(lo) | ((unsigned)f2bf(hi) << 16); }

DI unsigned wave_incl_scan(unsigned x) {
    x += __builtin_amdgcn_update_dpp(0u, x, 0x111, 0xf, 0xf, false);
    x += __builtin_amdgcn_update_dpp(0u, x, 0x112, 0xf, 0xf, false);
    x += __builtin_amdgcn_update_dpp(0u, x, 0x114, 0xf, 0xf, false);
    x += __builtin_amdgcn_update_dpp(0u, x, 0x118, 0xf, 0xf, false);
    x += __builtin_amdgcn_update_dpp(0u, x, 0x142, 0xa, 0xf, false);
    x += __builtin_amdgcn_update_dpp(0u, x, 0x143, 0xc, 0xf, false);
    return x;
}

__global__ __launch_bounds__(512) void k_part(const u32x4* __restrict__ src4, const u32x4* __restrict__ dst4,
                                              const u32x4* __restrict__ ea4, const unsigned short* __restrict__ uw,
                                              const unsigned short* __restrict__ vw, const unsigned short* __restrict__ gw,
                                              unsigned char* __restrict__ ws) {
    __shared__ __attribute__((aligned(16))) unsigned lds[CHUNK * 3 + 256];
    unsigned* rec = lds;
    unsigned* hist = lds + CHUNK * 3;
    const int tid = threadIdx.x, w = blockIdx.x, lane = tid & 63;
    const int q0 = w * QPW + tid, q1 = q0 + 512;
    const bool v0 = q0 < NQUAD, v1 = (tid + 512 < QPW) && (q1 < NQUAD);
    u32x4 d[2], s[2], e[2][2];
    {
        const __amdgpu_buffer_rsrc_t rd = __builtin_amdgcn_make_buffer_rsrc((void*)dst4, 0, NE * 4, 0x00020000);
        const __amdgpu_buffer_rsrc_t rs = __builtin_amdgcn_make_buffer_rsrc((void*)src4, 0, NE * 4, 0x00020000);
        const __amdgpu_buffer_rsrc_t re = __builtin_amdgcn_make_buffer_rsrc((void*)ea4, 0, NE * 8, 0x00020000);
        const unsigned o0 = q0 * 16, o1 = v1 ? q1 * 16 : 0x40000000u;
        d[0] = __builtin_amdgcn_raw_buffer_load_b128(rd, o0, 0, 2);
        d[1] = __builtin_amdgcn_raw_buffer_load_b128(rd, o1, 0, 2);
        s[0] = __builtin_amdgcn_raw_buffer_load_b128(rs, o0, 0, 2);
        e[0][0] = __builtin_amdgcn_raw_buffer_load_b128(re, 2 * o0, 0, 2);
        e[0][1] = __builtin_amdgcn_raw_buffer_load_b128(re, 2 * o0 + 16, 0, 2);
        s[1] = __builtin_amdgcn_raw_buffer_load_b128(rs, o1, 0, 2);
        e[1][0] = __builtin_amdgcn_raw_buffer_load_b128(re, 2 * o1, 0, 2);
        e[1][1] = __builtin_amdgcn_raw_buffer_load_b128(re, 2 * o1 + 16, 0, 2);
    }
    if (tid < 256) hist[tid] = 0;
    __syncthreads();
    unsigned bk[2][4], rk[2][4];
#pragma unroll
    for (int i = 0; i < 2; ++i)
        if (i == 0 ? v0 : v1) {
#pragma unroll
            for (int j = 0; j < 4; ++j) {
                bk[i][j] = __umul24(d[i][j], 42800u) >> 22;
                rk[i][j] = atomicAdd(&hist[bk[i][j]], 1u);
            }
        }
    __syncthreads();
    if (tid < 64) {
        u32x4 c = *(u32x4*)&hist[lane * 4];
        unsigned tot = c[0] + c[1] + c[2] + c[3];
        unsigned ex = wave_incl_scan(tot) - tot;
        u32x4 o = {ex, ex + c[0], ex + c[0] + c[1], ex + c[0] + c[1] + c[2]};
        *(u32x4*)&hist[lane * 4] = o;
        unsigned* tab = (unsigned*)(ws + WS_TAB);
#pragma unroll
        for (int j = 0; j < 4; ++j) tab[(lane * 4 + j) * NB + w] = o[j] | (c[j] << 16);
    }
    __syncthreads();
#pragma unroll
    for (int i = 0; i < 2; ++i)
        if (i == 0 ? v0 : v1) {
#pragma unroll
            for (int j = 0; j < 4; ++j) {
                unsigned pos = hist[bk[i][j]] + rk[i][j];
                unsigned dl = d[i][j] - bk[i][j] * NPB;
                unsigned* r = rec + pos * 3;
                r[0] = s[i][j] | (dl << 16);
                r[1] = e[i][j >> 1][(j & 1) * 2];
                r[2] = e[i][j >> 1][(j & 1) * 2 + 1];
            }
        }
    __syncthreads();
    u32x4* out = (u32x4*)(ws + WS_REC + (size_t)w * CHUNK * 12);
    constexpr int NV = CHUNK * 12 / 16;
#pragma unroll
    for (int i = 0; i < 5; ++i) {
        int k = tid + i * 512;
        if (k < NV) out[k] = *(u32x4*)&rec[k * 4];
    }
    if (tid >= 448 && w < 33) {
        const int c = lane & 31, h = lane >> 5;
        if (w < 32) {
            const int m = w >> 3, ct = (w >> 2) & 1, ks = w & 3;
            const unsigned short* W = m == 0 ? gw + 64 * 64 : (m == 1 ? vw : (m == 2 ? gw : uw));
            unsigned short v[8];
#pragma unroll
            for (int j = 0; j < 8; ++j) {
                unsigned short t = W[(16 * ks + 8 * h + j) * 64 + 2 * c + ct];
                v[j] = m == 0 ? f2bf(bf2f(t) * NLOG2E) : t;
            }
            u32x4 o;
#pragma unroll
            for (int j = 0; j < 4; ++j) o[j] = (unsigned)v[2 * j] | ((unsigned)v[2 * j + 1] << 16);
            *(u32x4*)(ws + WS_WF + (size_t)w * 1024 + lane * 16) = o;
        } else {
#pragma unroll
            for (int ct = 0; ct < 2; ++ct) {
                unsigned short v[4];
#pragma unroll
                for (int j = 0; j < 4; ++j) { unsigned short t = f2bf(bf2f(gw[(128 + j) * 64 + 2 * c + ct]) * NLOG2E); v[j] = h ? (unsigned short)0 : t; }
                u32x2 o = {(unsigned)v[0] | ((unsigned)v[1] << 16), (unsigned)v[2] | ((unsigned)v[3] << 16)};
                *(u32x2*)(ws + WS_WF + WF_C + ct * 512 + lane * 8) = o;
            }
        }
    }
}

constexpr int TMAX = 15;
constexpr int QCAP = 8 * TMAX * 8;
constexpr int L_EP = 0;
constexpr int L_AGG = NPB * 256;
constexpr int L_RING = L_AGG + (NPB + 1) * 256;
constexpr int L_E = L_RING + 65536;
constexpr int L_SRC = L_E + QCAP * 4 * 8;
constexpr int L_KEEP = L_SRC + QCAP * 4 * 2;
constexpr int L_META = L_KEEP + 8 * TMAX * 32;
constexpr int L_NODEQ = L_META + 8 * TMAX * 16;
constexpr int L_CNT = L_NODEQ + 1024;
constexpr int L_QB = L_CNT + 512;
constexpr int L_FLAG = L_QB + 512;
constexpr int L_TOTAL = L_FLAG + 16;
static_assert(L_TOTAL <= 163840, "LDS");
static_assert(L_RING % 16 == 0 && L_E % 16 == 0 && L_SRC % 16 == 0 && L_KEEP % 16 == 0 && L_META % 16 == 0 && L_CNT % 16 == 0, "align");

DI void dma4(u32x4 srd, unsigned ldsbase, unsigned a0, unsigned a1, unsigned a2, unsigned a3) {
    asm volatile(
        "s_mov_b32 m0, %1\n\ts_nop 0\n\t"
        "buffer_load_dwordx4 %2, %0, 0 offen lds\n\t"
        "s_add_u32 m0, m0, 1024\n\ts_nop 0\n\t"
        "buffer_load_dwordx4 %3, %0, 0 offen lds\n\t"
        "s_add_u32 m0, m0, 1024\n\ts_nop 0\n\t"
        "buffer_load_dwordx4 %4, %0, 0 offen lds\n\t"
        "s_add_u32 m0, m0, 1024\n\ts_nop 0\n\t"
        "buffer_load_dwordx4 %5, %0, 0 offen lds"
        :: "s"(srd), "s"(ldsbase), "v"(a0), "v"(a1), "v"(a2), "v"(a3) : "memory", "scc");
}

#define MFMA16(a, b, c) __builtin_amdgcn_mfma_f32_32x32x16_bf16((a), (b), (c), 0, 0, 0)
#define MFMA8(a, b, c) __builtin_amdgcn_mfma_f32_32x32x8bf16_1k((a), (b), (c), 0, 0, 0)

__global__ __launch_bounds__(512, 2) void k_main(const unsigned short* __restrict__ x, const unsigned char* __restrict__ ws,
                                                 unsigned short* __restrict__ out) {
    extern __shared__ __attribute__((aligned(16))) unsigned char lds[];
    const int tid = threadIdx.x, lane = tid & 63, b = blockIdx.x;
    const int wv = __builtin_amdgcn_readfirstlane(tid >> 6);
    const int c = lane & 31, h = lane >> 5;
    const unsigned ldsb = (unsigned)(size_t)lds;

    const u32x4* tabp = (const u32x4*)(ws + WS_TAB + (size_t)b * NB * 4 + (tid >> 4) * 32);
    u32x4 tb0 = tabp[0], tb1 = tabp[1];
    const unsigned tbo = *(const unsigned*)(ws + WS_TAB + (size_t)b * NB * 4 + (wv * 32 + c) * 4);

    u32x4 srd;
    {
        uint64_t p = (uint64_t)x;
        srd[0] = __builtin_amdgcn_readfirstlane((unsigned)p);
        srd[1] = __builtin_amdgcn_readfirstlane((unsigned)(p >> 32) & 0xffffu);
        srd[2] = NN * 128;
        srd[3] = 0x00020000;
    }
    const unsigned cb = (unsigned)(((lane & 7) ^ (((lane >> 4) & 1) << 1)) << 4);
    const unsigned cd0 = cb, cd1 = cb ^ 16, cd2 = cb ^ 64, cd3 = cb ^ 80;
    const unsigned ring = ldsb + L_RING + wv * 8192;
    const int rho = (c & 1) | ((c >> 1) & 2) | ((c >> 2) & 4) | ((c & 2) << 2) | ((c & 8) << 1);
    const int fsw = (c >> 1) & 7;
    unsigned aoff[4];
#pragma unroll
    for (int s = 0; s < 4; ++s) aoff[s] = L_RING + wv * 8192 + rho * 128 + (((2 * s + h) ^ fsw) << 4);

    {
        const int g = lane >> 3;
        const int rbase = (g & 1) | ((g & 2) << 1) | ((g & 4) << 2);
        const int n0 = b * NPB + (wv >> 1) * 32 + rbase;
        dma4(srd, ring, (unsigned)(n0 + 0) * 128 + cd0, (unsigned)(n0 + 2) * 128 + cd1, (unsigned)(n0 + 8) * 128 + cd2,
             (unsigned)(n0 + 10) * 128 + cd3);
    }
    const u32x4* wf = (const u32x4*)(ws + WS_WF) + lane;
    bf16x8 wa[4], wu[4];
#pragma unroll
    for (int s = 0; s < 4; ++s) {
        wa[s] = __builtin_bit_cast(bf16x8, wf[(16 + (wv & 1) * 4 + s) * 64]);
        wu[s] = __builtin_bit_cast(bf16x8, wf[(24 + (wv & 1) * 4 + s) * 64]);
    }
    {
        const u32x4 z = {0, 0, 0, 0}, f = {~0u, ~0u, ~0u, ~0u};
        for (int k = tid; k < (NPB + 1) * 16; k += 512) *(u32x4*)(lds + L_AGG + k * 16) = z;
        for (int k = tid; k < QCAP * 2; k += 512) *(u32x4*)(lds + L_E + k * 16) = z;
        if (tid < QCAP / 2) *(u32x4*)(lds + L_SRC + tid * 16) = f;
        if (tid < 32) *(u32x4*)(lds + L_CNT + tid * 16) = z;
        if (tid == 32) *(u32x4*)(lds + L_FLAG) = z;
    }
    const int l16 = tid & 15;
    u32x3 rc[9];
    bool ok[9];
    bool slow = false;
    {
        unsigned ent[8] = {tb0[0], tb0[1], tb0[2], tb0[3], tb1[0], tb1[1], tb1[2], tb1[3]};
        const __amdgpu_buffer_rsrc_t rrec = __builtin_amdgcn_make_buffer_rsrc((void*)(ws + WS_REC), 0, NB * CHUNK * 12, 0x00020000);
        const unsigned recb = (unsigned)((tid >> 4) * 8) * (CHUNK * 12) + l16 * 12;
#pragma unroll
        for (int i = 0; i < 8; ++i) {
            ok[i] = (unsigned)l16 < (ent[i] >> 16);
            unsigned o = recb + i * (CHUNK * 12) + (ent[i] & 0xffffu) * 12;
            rc[i] = __builtin_amdgcn_raw_buffer_load_b96(rrec, ok[i] ? o : 0x80000000u, 0, 0);
        }
        unsigned cn = tbo >> 16;
        unsigned ov = (h == 0 && cn > 16) ? cn - 16 : 0;
        unsigned inc = wave_incl_scan(ov);
        unsigned first = (unsigned)(wv * 32 + c) * CHUNK + (tbo & 0xffffu) + 16 - (inc - ov);
        unsigned tot = __builtin_amdgcn_readlane(inc, 63);
        unsigned long long m = __ballot(ov > 0);
        unsigned mine = 0;
        while (m) {
            int l = __builtin_ctzll(m);
            m &= m - 1;
            unsigned ex = __builtin_amdgcn_readlane(inc - ov, l), fi = __builtin_amdgcn_readlane(first, l);
            if ((unsigned)lane >= ex) mine = fi;
        }
        ok[8] = (unsigned)lane < tot;
        rc[8] = __builtin_amdgcn_raw_buffer_load_b96(rrec, ok[8] ? (mine + lane) * 12 : 0x80000000u, 0, 0);
        slow = tot > 64;
    }
    unsigned upk[8];
    {
        asm volatile("" :: "v"(wa[3]), "v"(wu[3]) : "memory");
        bf16x8 a[4];
#pragma unroll
        for (int s = 0; s < 4; ++s) a[s] = *(const bf16x8*)(lds + aoff[s]);
        f32x16 p = {0}, u = {0};
#pragma unroll
        for (int s = 0; s < 4; ++s) { p = MFMA16(a[s], wa[s], p); u = MFMA16(a[s], wu[s], u); }
#pragma unroll
        for (int i = 0; i < 16; ++i) {
            int row = (wv >> 1) * 32 + (i & 3) + 8 * (i >> 2) + 4 * h;
            if (row < NPB) *(float*)(lds + L_EP + row * 256 + c * 8 + (wv & 1) * 4) = __builtin_amdgcn_exp2f(p[i] * NLOG2E);
        }
#pragma unroll
        for (int i = 0; i < 8; ++i) upk[i] = pack_bf2(u[2 * i], u[2 * i + 1]);
    }
    bf16x8 wg[2][4], wvv[2][4];
    bf16x4 wc[2];
#pragma unroll
    for (int ct = 0; ct < 2; ++ct) {
#pragma unroll
        for (int s = 0; s < 4; ++s) {
            wg[ct][s] = __builtin_bit_cast(bf16x8, wf[(ct * 4 + s) * 64]);
            wvv[ct][s] = __builtin_bit_cast(bf16x8, wf[(8 + ct * 4 + s) * 64]);
        }
        wc[ct] = __builtin_bit_cast(bf16x4, *(const u32x2*)(ws + WS_WF + WF_C + ct * 512 + lane * 8));
    }
    __syncthreads();
    unsigned rk[9];
    unsigned* cnt = (unsigned*)(lds + L_CNT);
#pragma unroll
    for (int i = 0; i < 9; ++i)
        if (ok[i]) rk[i] = atomicAdd(&cnt[rc[i][0] >> 16], 1u);
    __syncthreads();
    unsigned Q;
    {
        u32x2 cc = *(u32x2*)(lds + L_CNT + lane * 8);
        unsigned a0 = (cc[0] + 3) >> 2, a1 = (cc[1] + 3) >> 2;
        unsigned inc = wave_incl_scan(a0 + a1);
        unsigned ex = inc - a0 - a1;
        u32x2 o = {ex * 4, (ex + a0) * 4};
        *(u32x2*)(lds + L_QB + lane * 8) = o;
        Q = __builtin_amdgcn_readlane(inc, 63);
    }
    const unsigned* sb = (const unsigned*)(lds + L_QB);
    auto put = [&](const u32x3& r, unsigned rank) {
        unsigned pos = sb[r[0] >> 16] + rank;
        *(u32x2*)(lds + L_E + pos * 8) = u32x2{r[1], r[2]};
        *(unsigned short*)(lds + L_SRC + pos * 2) = (unsigned short)r[0];
        *(unsigned char*)(lds + L_NODEQ + (pos >> 2)) = (unsigned char)(r[0] >> 16);
    };
    if (Q <= QCAP) {
#pragma unroll
        for (int i = 0; i < 9; ++i)
            if (ok[i]) put(rc[i], rk[i]);
    } else {
        Q = 0;
    }
    __syncthreads();

    const unsigned S = (Q + 31) >> 5, T1 = (S + 1) >> 1, T2 = S >> 1;
    const unsigned T = wv < 4 ? T1 : T2, L = 2 * T;
    const unsigned wbase = wv < 4 ? wv * 4 * L : 32 * T1 + (wv - 4) * 4 * L;
    unsigned srca;
    {
        const int g = lane >> 3;
        srca = L_SRC + (wbase + (g & 3) * L + (g >> 2)) * 8;
    }
    unsigned ea;
    {
        const int kk = (c & 1) | ((c >> 1) & 2), j = ((c >> 1) & 1) | ((c >> 2) & 2), u = c >> 4;
        ea = L_E + ((wbase + kk * L + u) * 4 + j) * 8;
    }
    auto issue = [&](unsigned t, unsigned slot) {
        u32x2 sv = *(const u32x2*)(lds + srca + t * 16);
        unsigned a0 = (sv[0] & 0xffffu) * 128 + cd0, a1 = (sv[0] >> 16) * 128 + cd1;
        unsigned a2 = (sv[1] & 0xffffu) * 128 + cd2, a3 = (sv[1] >> 16) * 128 + cd3;
        dma4(srd, ring + slot * 4096, a0, a1, a2, a3);
    };
    if (T > 0) issue(0, 0);
    if (T > 1) issue(1, 1);
    {
        const unsigned char* nq = lds + L_NODEQ;
#pragma unroll
        for (int it = 0; it < 2; ++it) {
            const unsigned kk = (lane >> 5) + 2 * it, p = lane & 31;
            if (p < L) {
                const unsigned s0 = wbase + kk * L, q = s0 + p, se = s0 + L;
                unsigned node = 0, ag = NPB;
                float keep = 1.0f;
                if (q < Q) {
                    node = nq[q];
                    ag = node;
                    if (p > 0 && nq[q - 1] != node) keep = 0.0f;
                    if (se < Q && nq[se] == node) ag = NPB;
                }
                const unsigned t = p >> 1, u = p & 1, X = 2 * u + (kk & 1), hh = kk >> 1;
                const unsigned th = (wv * TMAX + t) * 2 + hh;
                *(float*)(lds + L_KEEP + th * 16 + X * 4) = keep;
                lds[L_META + th * 8 + X] = (unsigned char)node;
                lds[L_META + th * 8 + 4 + X] = (unsigned char)ag;
            }
        }
    }
    const unsigned c8 = c * 8;
    f32x2 acc[2] = {{0.f, 0.f}, {0.f, 0.f}};
    for (unsigned t = 0; t < T; ++t) {
        const unsigned slot = t & 1;
        if (t + 1 < T) asm volatile("s_waitcnt vmcnt(4)" ::: "memory");
        else asm volatile("s_waitcnt vmcnt(0)" ::: "memory");
        bf16x8 a[4];
#pragma unroll
        for (int s = 0; s < 4; ++s) a[s] = *(const bf16x8*)(lds + aoff[s] + slot * 4096);
        bf16x4 ef = *(const bf16x4*)(lds + ea + t * 64);
        const unsigned th = (wv * TMAX + t) * 2 + h;
        u32x2 mt = *(const u32x2*)(lds + L_META + th * 8);
        f32x4 kp = *(const f32x4*)(lds + L_KEEP + th * 16);
        asm volatile("s_waitcnt lgkmcnt(0)" : "+v"(a[0]), "+v"(a[1]), "+v"(a[2]), "+v"(a[3]) :: "memory");
        if (t + 2 < T) issue(t + 2, slot);
        f32x2 ep[4];
        unsigned ga[4];
#pragma unroll
        for (int X = 0; X < 4; ++X) {
            ep[X] = *(const f32x2*)(lds + L_EP + (((mt[0] >> (8 * X)) & 0xffu) << 8) + c8);
            ga[X] = L_AGG + (((mt[1] >> (8 * X)) & 0xffu) << 8) + c8;
        }
#pragma unroll
        for (int ct = 0; ct < 2; ++ct) {
            f32x16 z = {0}, hv = {0};
            z = MFMA8(ef, wc[ct], z);
#pragma unroll
            for (int s = 0; s < 4; ++s) { z = MFMA16(a[s], wg[ct][s], z); hv = MFMA16(a[s], wvv[ct][s], hv); }
#pragma unroll
            for (int u = 0; u < 2; ++u) {
                f32x2 d[4], hh[4];
#pragma unroll
                for (int j = 0; j < 4; ++j) {
                    const int i = 8 * u + 2 * j;
                    d[j][0] = __builtin_fmaf(__builtin_amdgcn_exp2f(z[i]), ep[2 * u][ct], 1.0f);
                    d[j][1] = __builtin_fmaf(__builtin_amdgcn_exp2f(z[i + 1]), ep[2 * u + 1][ct], 1.0f);
                    hh[j] = f32x2{hv[i], hv[i + 1]};
                }
                f32x2 d01 = d[0] * d[1], d23 = d[2] * d[3];
                f32x2 n01 = __builtin_elementwise_fma(hh[1], d[0], hh[0] * d[1]);
                f32x2 n23 = __builtin_elementwise_fma(hh[3], d[2], hh[2] * d[3]);
                f32x2 den = d01 * d23;
                f32x2 num = __builtin_elementwise_fma(n23, d01, n01 * d23);
                f32x2 r = {__builtin_amdgcn_rcpf(den[0]), __builtin_amdgcn_rcpf(den[1])};
                f32x2 k2 = {kp[2 * u], kp[2 * u + 1]};
                acc[ct] = __builtin_elementwise_fma(num, r, acc[ct] * k2);
                *(float*)(lds + ga[2 * u] + ct * 4) = acc[ct][0];
                *(float*)(lds + ga[2 * u + 1] + ct * 4) = acc[ct][1];
            }
        }
    }
    {
        unsigned char* rg = lds + L_RING + wv * 8192;
#pragma unroll
        for (int v = 0; v < 2; ++v) *(f32x2*)(rg + (2 * h + v) * 256 + c8) = f32x2{acc[0][v], acc[1][v]};
#pragma unroll
        for (int i = 0; i < 16; ++i) {
            int row = (i & 3) + 8 * (i >> 2) + 4 * h;
            *(unsigned*)(rg + 4096 + row * 128 + c * 4) = (i & 1) ? (upk[i >> 1] & 0xffff0000u) : (upk[i >> 1] << 16);
        }
    }
    __syncthreads();
    {
        const int n = tid >> 2, part = tid & 3;
        const int node = b * NPB + n;
        if (n < NPB && node < NN) {
            float v[16];
#pragma unroll
            for (int k = 0; k < 4; ++k) {
                f32x4 g = *(const f32x4*)(lds + L_AGG + n * 256 + part * 64 + k * 16);
#pragma unroll
                for (int j = 0; j < 4; ++j) v[k * 4 + j] = g[j];
            }
            const unsigned qs = sb[n] >> 2, qe = sb[n + 1] >> 2;
            if (qe > qs && T1 > 0) {
                const unsigned L1 = 2 * T1, L2 = 2 * T2, B2 = 16 * L1;
                unsigned sf = qs < B2 ? qs / L1 : 16 + (qs - B2) / L2;
                unsigned sl = (qe - 1) < B2 ? (qe - 1) / L1 : 16 + (qe - 1 - B2) / L2;
                for (unsigned s = sf; s < sl; ++s) {
                    const unsigned char* tp = lds + L_RING + (s >> 2) * 8192 + (s & 3) * 256 + part * 64;
#pragma unroll
                    for (int k = 0; k < 4; ++k) {
                        f32x4 g = *(const f32x4*)(tp + k * 16);
#pragma unroll
                        for (int j = 0; j < 4; ++j) v[k * 4 + j] += g[j];
                    }
                }
            }
            const unsigned char* u0 = lds + L_RING + ((n >> 5) * 2) * 8192 + 4096 + (n & 31) * 128 + part * 32;
#pragma unroll
            for (int k = 0; k < 2; ++k) {
                f32x4 e0 = *(const f32x4*)(u0 + k * 16), e1 = *(const f32x4*)(u0 + 8192 + k * 16);
#pragma unroll
                for (int j = 0; j < 4; ++j) { v[k * 8 + 2 * j] += e0[j]; v[k * 8 + 2 * j + 1] += e1[j]; }
            }
            unsigned o[8];
#pragma unroll
            for (int k = 0; k < 8; ++k) {
                float a0 = v[2 * k], a1 = v[2 * k + 1];
                a0 = a0 * __builtin_amdgcn_rcpf(1.0f + __builtin_amdgcn_exp2f(a0 * NLOG2E));
                a1 = a1 * __builtin_amdgcn_rcpf(1.0f + __builtin_amdgcn_exp2f(a1 * NLOG2E));
                o[k] = pack_bf2(a0, a1);
            }
            u32x4* op = (u32x4*)(out + (size_t)node * 64 + part * 16);
            op[0] = u32x4{o[0], o[1], o[2], o[3]};
            op[1] = u32x4{o[4], o[5], o[6], o[7]};
        }
    }
}

extern "C" void kernel_launch(void* const* d_in, const int* in_sizes, int n_in, void* d_out, int out_size, void* d_ws,
                              size_t ws_size, hipStream_t stream) {
    const unsigned short* x = (const unsigned short*)d_in[0];
    const u32x4* ea = (const u32x4*)d_in[1];
    const u32x4* src = (const u32x4*)d_in[2];
    const u32x4* dst = (const u32x4*)d_in[3];
    const unsigned short* uw = (const unsigned short*)d_in[4];
    const unsigned short* vw = (const unsigned short*)d_in[5];
    const unsigned short* gw = (const unsigned short*)d_in[6];
    (void)hipFuncSetAttribute((const void*)k_main, hipFuncAttributeMaxDynamicSharedMemorySize, L_TOTAL);
    k_part<<<NB, 512, 0, stream>>>(src, dst, ea, uw, vw, gw, (unsigned char*)d_ws);
    k_main<<<NB, 512, L_TOTAL, stream>>>(x, (const unsigned char*)d_ws, (unsigned short*)d_out);
}
```

```cpp
#include <hip/hip_runtime.h>
#include <hip/hip_bf16.h>
#include <stdint.h>
#pragma clang diagnostic ignored "-Wint-to-pointer-cast"

typedef __attribute__((ext_vector_type(4))) unsigned u32x4;
typedef __attribute__((ext_vector_type(2))) unsigned u32x2;
typedef __attribute__((ext_vector_type(8))) unsigned u32x8;
typedef __attribute__((ext_vector_type(32))) unsigned u32x32;
typedef __attribute__((ext_vector_type(8))) short bf16x8;
typedef __attribute__((ext_vector_type(16))) float f32x16;
typedef __attribute__((ext_vector_type(2))) float f32x2;
typedef __attribute__((address_space(1))) unsigned long long gu64;
typedef __attribute__((address_space(1))) unsigned gu32;

#define DI __device__ __forceinline__
#define BAR() asm volatile("s_waitcnt lgkmcnt(0)\n\ts_barrier" ::: "memory")
#define RLX_AGENT __ATOMIC_RELAXED, __HIP_MEMORY_SCOPE_AGENT

constexpr int PSLEEP = 2, KSLEEP = 4;
constexpr int CODEBYTES = 14500;
constexpr int HOLD = 320;
constexpr int OAUX = 16;
constexpr int NN = 25000;
constexpr int NE = 800000;
constexpr int NB = 256;
constexpr int NPB = 98;
constexpr int PMAIN = 1536, PX = 27;
constexpr int PREST = NE / 2 - NB * PMAIN;
static_assert(PREST >= 0 && PREST <= NB * PX, "slices");

constexpr size_t WS_EPOCH = 0;
constexpr int FCH = NB * 128 + 128;
constexpr int KCH = 16384 + 128;
constexpr int EUSE = 1024 + 3136 * 8, ECH = EUSE + 128;
constexpr size_t WS_FLG = 256;
constexpr size_t WS_KEY = WS_FLG + 8 * FCH;
constexpr size_t WS_OK = WS_KEY + (size_t)NB * KCH;
constexpr size_t WS_EV = WS_OK + (size_t)NB * KCH;
constexpr unsigned WS_END = (unsigned)(WS_EV + (size_t)NB * ECH + 128);
constexpr unsigned TAGM = 0xFE038000u;
constexpr unsigned TAGH = 0xE0038000u;

constexpr float NLOG2E = -1.4426950408889634f;
constexpr float VSCALE = 281474976710656.0f;
constexpr float VUNSCALE = 1.0f / 281474976710656.0f;

constexpr int TMAX = 14;
constexpr int QCAP = 8 * TMAX * 8;
constexpr int L_CNT = 0;
constexpr int L_ZR = 512;
constexpr int L_QB = 768;
constexpr int L_AGG = 1280;
constexpr int L_PB = L_AGG + 27264;
constexpr int L_META = L_PB + (NPB + 1) * 128;
constexpr int L_EI = L_META + 2048;
constexpr int EI_OVF = 32768, EI_ZERO = 32768 + 511 * 8;
constexpr int L_RING = L_EI + 36864;
constexpr int L_KEY = L_RING + 65536;
constexpr int L_TP = L_KEY + QCAP * 16;
constexpr int L_TOTAL = L_TP + 512;
static_assert(L_TOTAL <= 163840, "LDS");
static_assert(L_PB % 16 == 0 && L_EI % 16 == 0 && L_KEY % 16 == 0 && L_META % 16 == 0 && L_PB == 28544 && L_EI == 43264 && L_RING % 128 == 0, "align");
constexpr int L_SOK = L_PB + 64;
constexpr int L_HIST = L_AGG + 4096;
constexpr int L_SK = L_AGG + 8192;
constexpr int L_OFF = L_EI;
constexpr int L_SE = L_EI + 1024;
constexpr int L_BIN = L_AGG;
constexpr int L_OWN = L_RING + 4 * 8192 + 4096;
constexpr int L_CW = L_RING + 4 * 8192;
constexpr unsigned PADKEY = 0xffffu | (EI_ZERO << 16);
constexpr int L_MULTI = L_ZR - 8;
constexpr int L_SLOW = L_ZR - 4;
constexpr unsigned BIG = 0x100000u;

DI float bf2f(unsigned short v) { return __uint_as_float((unsigned)v << 16); }
typedef __attribute__((ext_vector_type(2))) __bf16 bf16x2;
DI unsigned pack_bf2(float lo, float hi) { return __builtin_bit_cast(unsigned, __builtin_convertvector(f32x2{lo, hi}, bf16x2)); }
DI float lo_f(unsigned v) { return __uint_as_float(v << 16); }
DI float hi_f(unsigned v) { return __uint_as_float(v & 0xffff0000u); }

DI unsigned wave_incl_scan(unsigned x) {
    x += __builtin_amdgcn_update_dpp(0u, x, 0x111, 0xf, 0xf, false);
    x += __builtin_amdgcn_update_dpp(0u, x, 0x112, 0xf, 0xf, false);
    x += __builtin_amdgcn_update_dpp(0u, x, 0x114, 0xf, 0xf, false);
    x += __builtin_amdgcn_update_dpp(0u, x, 0x118, 0xf, 0xf, false);
    x += __builtin_amdgcn_update_dpp(0u, x, 0x142, 0xa, 0xf, false);
    x += __builtin_amdgcn_update_dpp(0u, x, 0x143, 0xc, 0xf, false);
    return x;
}

DI unsigned wave_incl_max(unsigned x) {
    x = max(x, (unsigned)__builtin_amdgcn_update_dpp(0u, x, 0x111, 0xf, 0xf, false));
    x = max(x, (unsigned)__builtin_amdgcn_update_dpp(0u, x, 0x112, 0xf, 0xf, false));
    x = max(x, (unsigned)__builtin_amdgcn_update_dpp(0u, x, 0x114, 0xf, 0xf, false));
    x = max(x, (unsigned)__builtin_amdgcn_update_dpp(0u, x, 0x118, 0xf, 0xf, false));
    x = max(x, (unsigned)__builtin_amdgcn_update_dpp(0u, x, 0x142, 0xa, 0xf, false));
    x = max(x, (unsigned)__builtin_amdgcn_update_dpp(0u, x, 0x143, 0xc, 0xf, false));
    return x;
}

DI u32x4 mk_srd(const void* ptr, unsigned bytes) {
    const uint64_t p = (uint64_t)ptr;
    return u32x4{(unsigned)__builtin_amdgcn_readfirstlane((unsigned)p),
                 (unsigned)__builtin_amdgcn_readfirstlane((unsigned)(p >> 32) & 0xffffu), bytes, 0x00020000u};
}

#define LD16(dst_, off_, srd_, imm_) asm volatile("buffer_load_dwordx4 %0, %1, %2, 0 offen offset:" #imm_ : "=v"(dst_) : "v"(off_), "s"(srd_) : "memory")
#define LAS __attribute__((address_space(3)))
#define LDS(T, a) (*(LAS T*)(unsigned)(a))
#define LADD(a, v) __hip_atomic_fetch_add((LAS unsigned*)(unsigned)(a), (v), __ATOMIC_RELAXED, __HIP_MEMORY_SCOPE_WORKGROUP)
#define ST16(v_, off_, so_, imm_) asm volatile("buffer_store_dwordx4 %0, %1, %2, %3 offen offset:%4 sc1\n\ts_nop 1" :: "v"(v_), "v"(off_), "s"(rws), "s"(so_), "n"(imm_) : "memory")
#define MFMA16(a, b, c) __builtin_amdgcn_mfma_f32_32x32x16_bf16((a), (b), (c), 0, 0, 0)
#define RSRC(p, n) __builtin_amdgcn_make_buffer_rsrc((void*)(p), 0, (n), 0x00020000)

__global__ __launch_bounds__(512, 2) void k_egc(const unsigned short* __restrict__ x, const unsigned* __restrict__ ea,
                                                const unsigned* __restrict__ src, const unsigned* __restrict__ dst,
                                                const unsigned* __restrict__ uw, const unsigned* __restrict__ vw,
                                                const unsigned* __restrict__ gw, unsigned char* ws,
                                                unsigned short* __restrict__ out) {
    extern __shared__ __attribute__((aligned(128))) unsigned char lds[];
    const int tid = threadIdx.x, lane = tid & 63, w = blockIdx.x;
    const int b = (w & 7) * 32 + (w >> 3);
    const int wv = __builtin_amdgcn_readfirstlane(tid >> 6);
    const int c = lane & 31, h = lane >> 5;
    asm volatile("" :: "s"(x), "s"(ea), "s"(src), "s"(dst), "s"(uw), "s"(vw), "s"(gw), "s"(ws), "s"(out));
    const unsigned long long t_start = __builtin_amdgcn_s_memrealtime();
    const bool v1 = lane < PX && w * PX + lane < PREST;
    const unsigned o0 = (w * PMAIN + wv * 192 + lane) * 8, o1 = v1 ? (NB * PMAIN + w * PX + lane) * 8 : 0x40000000u;
    u32x2 d[4], s[4];
    u32x4 e[4];
#define LD8(dst_, off_, srd_, imm_) asm volatile("buffer_load_dwordx2 %0, %1, %2, 0 offen offset:" #imm_ : "=v"(dst_) : "v"(off_), "s"(srd_) : "memory")
    {
        const u32x4 rd = mk_srd(dst, NE * 4);
        LD8(d[0], o0, rd, 0); LD8(d[1], o0, rd, 512); LD8(d[2], o0, rd, 1024);
        LD8(d[3], o1, mk_srd(dst, wv == 3 ? NE * 4 : 0), 0);
    }
    {
        unsigned long long pc;
        asm volatile("s_getpc_b64 %0" : "=s"(pc));
        const u32x4 rc = mk_srd(wv == 0 ? (const void*)pc : (wv == 1 ? (const void*)gw : (wv == 2 ? (const void*)vw : (const void*)uw)),
                                wv == 0 ? CODEBYTES : (wv == 1 ? 16896 : (wv < 4 ? 8192 : 0)));
        asm volatile("s_mov_b32 m0, %2\n\ts_nop 0\n\tbuffer_load_dword %0, %1, 0 offen lds"
                     :: "v"(lane < 5 ? ((w >> 3) * 5 + lane) * 128 : 0x40000000), "s"(rc), "s"(L_KEY) : "memory");
    }
    LDS(unsigned, tid * 4 + (wv < 4 ? L_HIST : L_CNT - 1024)) = wv < 4 ? ((tid << 3) & 48) + 4 : (tid < 256 + NPB ? (tid - 256) * 272 : (tid < 256 + NPB + 16 ? BIG : 0));
    {
        const unsigned em = ((NPB + ((tid + 4 * w) & 15)) << 18) | 0x7fffu;
        LDS(u32x4, L_SK + tid * 16) = u32x4{em, em, em, em};
        LDS(u32x4, L_SK + 8192 + tid * 16) = u32x4{em, em, em, em};
    }
    BAR();
    {
        const u32x4 re = mk_srd(ea, NE * 8);
        LD16(e[0], 2 * o0, re, 0); LD16(e[1], 2 * o0, re, 1024); LD16(e[2], 2 * o0, re, 2048);
        LD16(e[3], 2 * o1, mk_srd(ea, wv == 3 ? NE * 8 : 0), 0);
    }
    const u32x4 rws = mk_srd(ws, WS_END);
    u32x2 epv;
    asm volatile("buffer_load_dwordx2 %0, off, %1, 0 sc1" : "=v"(epv) : "s"(rws) : "memory");
    unsigned so_ko = WS_KEY + w * KCH + wv * 2048, so_eo = WS_EV + w * ECH + wv * 3072;
    unsigned so_oo = wv < 2 ? WS_EV + w * ECH + 24576 + wv * 1024 : WS_OK + w * KCH;
    unsigned so_ki = WS_KEY + wv * 32 * KCH + b * 64, so_ei = WS_EV + wv * 32 * ECH;
    unsigned so_ok = WS_OK + wv * 32 * KCH, so_fl = WS_FLG + (w & 7) * FCH + wv * 32 * 128;
    u32x4 rown = mk_srd(x, min(b * NPB + 104, NN) * 128);
    asm volatile("" : "+s"(so_ko), "+s"(so_eo), "+s"(so_oo), "+s"(so_ki), "+s"(so_ei), "+s"(so_ok), "+s"(so_fl), "+s"(rown));
    unsigned vbig = BIG, l16 = lane * 16;
    u32x4 rw = mk_srd((wv < 2 ? gw + 64 * 32 : (wv < 4 ? vw : (wv < 6 ? gw : uw))) + (wv & 1) * 1024, wv == 1 ? 4608 : 4096);
    asm volatile("" : "+v"(vbig), "+v"(l16));
    asm volatile("" : "+s"(rw));
    const u32x4 srd = mk_srd(x, NN * 128);
    const unsigned cb = (unsigned)(((lane & 7) ^ (((lane >> 4) & 1) << 1)) << 4);
    u32x8 dpr = {0, cb, 0, cb ^ 16, 0, cb ^ 64, 0, cb ^ 80};
    const unsigned ring = L_RING + wv * 8192;
    unsigned c8 = c * 8;
    unsigned ua, pa, own0;
    {
        const unsigned ur = wv * 14 + ((c >> 2) & 1) * 7 + ((c & 3) | ((c >> 1) & 4));
        ua = L_OWN + (ur >> 5) * 8192 + (ur & 31) * 128 + ((h ^ ((ur >> 1) & 7)) << 4);
        pa = L_OWN + (wv >> 1) * 8192 + c * 128 + ((h ^ ((c >> 1) & 7)) << 4);
        own0 = (b * NPB + wv * 16 + (lane >> 3)) * 128 + (((lane & 7) ^ (lane >> 4)) << 4);
    }
    asm volatile("" : "+v"(dpr), "+v"(c8), "+v"(ua), "+v"(pa), "+v"(own0));
    const int j4 = lane & 3;
    unsigned eb = (unsigned)(wv * 4096 + (lane >> 2) * 128 + j4 * 32 - 8) << 16, hm = j4 ? TAGM : TAGH;
    unsigned wfr = L_RING + l16, eo = (lane >> 3) * ECH + 1024 + (lane & 7) * 16, hdm = NPB * 4 + (lane & 60);
    unsigned ko = (lane >> 2) * KCH + ((j4 + (b >> 1)) & 3) * 16, of0 = ((lane >> 2) + 16 * j4) * (KCH / 4), of1 = ((lane >> 2) + 16 * j4) * (ECH / 8) + 128 + 15;
    unsigned bb = lane * 544;
    asm volatile("" : "+v"(bb));
    unsigned scr = L_META + wv * 256 + lane, pmw = L_META + wv * 256 + (lane >> 3) * 32 + (lane & 7) * 2;
    unsigned ks = (((lane >> 2) & 4) | ((lane >> 4) & 2) | ((lane >> 3) & 1)) * 16;
    if (tid < 128) LDS(unsigned, L_TP + tid * 4) = L_ZR;
    unsigned pb = (wv >> 1) * 32 + c <= NPB ? ((wv >> 1) * 32 + c) * 128 + (wv & 1) * 64 + ((h ^ (c & 7)) << 3) : BIG;
    asm volatile("" : "+v"(pb));
    u32x4 so_e4 = {so_ei, so_ei + 8 * ECH - 1024, so_ei + 16 * ECH - 2048, so_ei + 24 * ECH - 3072};
    unsigned so_k2 = so_ki + 16 * KCH;
    LDS(unsigned short, L_META + wv * 256 + lane * 2) = 0;
    asm volatile("" : "+v"(eb), "+v"(hm), "+v"(wfr), "+v"(eo), "+v"(hdm), "+v"(ko), "+v"(of0), "+v"(of1), "+v"(scr), "+v"(pmw), "+v"(ks));
    asm volatile("" : "+s"(so_e4), "+s"(so_k2));
    unsigned pf0 = lane < 13 ? (b * NPB + wv * 13 + lane) * 128 : 0x40000000;
    asm volatile("" : "+v"(pf0));
    {
        const u32x4 rs = mk_srd(src, NE * 4);
        LD8(s[0], o0, rs, 0); LD8(s[1], o0, rs, 512); LD8(s[2], o0, rs, 1024);
        LD8(s[3], o1, mk_srd(src, wv == 3 ? NE * 4 : 0), 0);
    }
    unsigned b4[4][2], rk[4][2];
    asm volatile("s_waitcnt vmcnt(10)" : "+v"(d[0]), "+v"(d[1]), "+v"(d[2]), "+v"(d[3]) :: "memory");
#pragma unroll
    for (int i = 0; i < 4; ++i)
        if (i < 3 || wv == 3) {
            const unsigned inv = (i < 3 || v1) ? 0u : BIG;
#pragma unroll
            for (int j = 0; j < 2; ++j) {
                b4[i][j] = ((__umul24(d[i][j], 42800u) >> 20) & ~3u) | inv;
                rk[i][j] = LADD(L_HIST + b4[i][j], 0x40004u);
            }
        }
    asm volatile("" ::: "memory");
    BAR();
    asm volatile("s_mov_b32 m0, %2\n\ts_nop 0\n\tbuffer_load_dword %0, %1, 0 offen lds" :: "v"(pf0), "s"(srd), "s"(L_KEY) : "memory");
    unsigned ad[4][2], ka[4][2];
#pragma unroll
    for (int i = 0; i < 4; ++i)
        if (i < 3 || wv == 3) {
#pragma unroll
            for (int j = 0; j < 2; ++j) {
                d[i][j] = d[i][j] * 4 - __umul24(b4[i][j], NPB);
                ka[i][j] = ((b4[i][j] << 4) | (rk[i][j] & 60u)) + L_SK;
            }
        }
    unsigned otot;
    {
        const u32x4 cc = LDS(u32x4, L_HIST + l16);
        u32x4 oc;
#pragma unroll
        for (int j = 0; j < 4; ++j) oc[j] = (cc[j] >> 16) | (__builtin_elementwise_sub_sat(cc[j] >> 16, 60u) << 16);
        const unsigned t1 = oc[0] + oc[1], tot = t1 + oc[2] + oc[3];
        const unsigned inc = wave_incl_scan(tot), ex = inc - tot;
        LDS(u32x4, L_OFF + l16) = u32x4{ex, ex + oc[0], ex + t1, ex + t1 + oc[2]};
        otot = __builtin_amdgcn_readlane(inc, 63) >> 16;
    }
    if (wv >= 4) {
        const unsigned t2 = tid - 256;
        const unsigned cn = LDS(unsigned, L_HIST + t2 * 4) >> 18, of = LDS(unsigned, L_OFF + t2 * 4);
        LDS(unsigned, L_SK + t2 * 64 + ((t2 << 3) & 48)) = min(cn, 63u) | (min(of >> 18, 255u) << 6) | ((of & 4) << 12) | ((of & 0xfff8u) << 15);
    }
    {
#pragma unroll
        for (int i = 0; i < 3; ++i)
#pragma unroll
            for (int j = 0; j < 2; ++j) b4[i][j] = LDS(unsigned, L_OFF + b4[i][j]);
        asm volatile("" : "+v"(b4[0][0]), "+v"(b4[0][1]), "+v"(b4[1][0]), "+v"(b4[1][1]), "+v"(b4[2][0]), "+v"(b4[2][1]));
#pragma unroll
        for (int i = 0; i < 4; ++i)
            if (i < 3 || wv == 3) {
                if (i == 3) {
                    b4[3][0] = LDS(unsigned, L_OFF + b4[3][0]); b4[3][1] = LDS(unsigned, L_OFF + b4[3][1]);
                    asm volatile("" : "+v"(b4[3][0]), "+v"(b4[3][1]));
                }
#pragma unroll
                for (int j = 0; j < 2; ++j) {
                    ad[i][j] = (((b4[i][j] & 0xffffu) + (rk[i][j] >> 16)) << 1) | ((i < 3 || v1) ? 0u : BIG);
                    ka[i][j] = rk[i][j] >= 0x3C0000u ? (b4[i][j] >> 16) + (rk[i][j] >> 16) + (L_SOK - 60) : ka[i][j];
                }
            }
    }
    asm volatile("" : "+v"(ad[0][0]), "+v"(ad[0][1]), "+v"(ad[1][0]), "+v"(ad[1][1]), "+v"(ad[2][0]), "+v"(ad[2][1]), "+v"(ka[0][0]), "+v"(ka[0][1]),
                      "+v"(ka[1][0]), "+v"(ka[1][1]), "+v"(ka[2][0]), "+v"(ka[2][1]), "+v"(d[0]), "+v"(d[1]), "+v"(d[2]));
    unsigned oov = (tid * 16 < EUSE - 24576 || wv == 2) ? l16 : 0x40000000u;
    unsigned omask = wv == 2 ? ~TAGM : ~0u, sea = L_OFF + wv * 3072 + l16, seb = (wv < 2 ? L_OFF + 24576 + wv * 1024 : L_SOK) + l16;
    unsigned ska = L_SK + wv * 2048 + l16, t0 = (lane & 3) == ((lane >> 3) & 3) ? TAGH : TAGM;
    asm volatile("" : "+v"(oov), "+v"(omask), "+v"(sea), "+v"(seb), "+v"(ska), "+v"(t0));
    asm volatile("s_waitcnt vmcnt(5)" : "+v"(e[0]), "+v"(e[1]), "+v"(e[2]), "+v"(e[3]), "+v"(epv) :: "memory");
#pragma unroll
    for (int i = 0; i < 4; ++i)
        if (i < 3 || wv == 3) {
#pragma unroll
            for (int j = 0; j < 2; ++j) LDS(u32x2, L_SE + ad[i][j]) = u32x2{e[i][2 * j], e[i][2 * j + 1]};
        }
    __builtin_amdgcn_sched_barrier(0);
    const unsigned long long epoch = ((unsigned long long)(unsigned)__builtin_amdgcn_readfirstlane(epv[1]) << 32) | (unsigned)__builtin_amdgcn_readfirstlane(epv[0]);
    unsigned tg[4];
    const unsigned long long next_epoch = (((epoch & 0x03FE03FE03FE03FEull) + 0x0002000200020002ull) & 0x03FE03FE03FE03FEull) |
                                          (epoch & 0x0001000000000001ull) | 0x10000ull;
#pragma unroll
    for (int k = 0; k < 4; ++k) {
        const unsigned f = (unsigned)(next_epoch >> (16 * k)) & 0x3ffu;
        tg[k] = ((f << 22) | (f << 15)) & TAGM;
    }
    u32x4 otag = {wv == 2 ? tg[0] : 0, wv == 2 ? tg[1] : 0, wv == 2 ? tg[2] : 0, wv == 2 ? tg[3] : 0};
    unsigned tg0 = tg[0] & hm;
    t0 &= tg[0];
    asm volatile("" : "+v"(tg0), "+v"(t0));
    asm volatile("" : "+s"(otag), "+s"(tg[0]), "+s"(tg[1]), "+s"(tg[2]), "+s"(tg[3]));
    asm volatile("s_waitcnt vmcnt(1)" : "+v"(s[0]), "+v"(s[1]), "+v"(s[2]), "+v"(s[3]) :: "memory");
#pragma unroll
    for (int i = 0; i < 4; ++i) { s[i][0] |= d[i][0] << 16; s[i][1] |= d[i][1] << 16; }
    asm volatile("" : "+v"(s[0]), "+v"(s[1]), "+v"(s[2]), "+v"(s[3]));
    if (__builtin_expect(otot > 1024, 0)) {
        const __amdgpu_buffer_rsrc_t r1 = RSRC(ws + WS_OK + (size_t)w * KCH, 16384);
#pragma unroll
        for (int i = 0; i < 4; ++i)
            if (i < 3 || wv == 3) {
#pragma unroll
                for (int j = 0; j < 2; ++j) {
                    __builtin_amdgcn_raw_buffer_store_b32(s[i][j], r1, ka[i][j] >= L_SOK + 1024 ? ka[i][j] - L_SOK : BIG, 0, 16);
                    ka[i][j] = ka[i][j] >= L_SOK + 1024 ? BIG : ka[i][j];
                }
            }
    }
#pragma unroll
    for (int i = 0; i < 4; ++i)
        if (i < 3 || wv == 3) {
#pragma unroll
            for (int j = 0; j < 2; ++j) LDS(unsigned, ka[i][j]) = s[i][j];
        }
    asm volatile("s_mov_b32 m0, %1\n\ts_nop 0\n\t"
                 "buffer_load_dwordx4 %2, %0, 0 offen lds\n\tbuffer_load_dwordx4 %2, %0, 0 offen offset:1024 lds\n\t"
                 "buffer_load_dwordx4 %2, %0, 0 offen offset:2048 lds\n\tbuffer_load_dwordx4 %2, %0, 0 offen offset:3072 lds\n\t"
                 "s_mov_b32 m0, %3\n\ts_nop 0\n\tbuffer_load_dwordx4 %4, %0, 0 offen lds\n\t"
                 "s_mov_b32 m0, %6\n\ts_nop 0\n\tbuffer_load_dwordx4 %7, %5, 0 offen lds\n\tbuffer_load_dwordx4 %8, %5, 0 offen offset:1024 lds"
                 :: "s"(rw), "s"(L_RING + (wv & 3) * 8192 + (wv >> 2) * 4096), "v"(l16), "s"(L_CW + (wv == 1 ? 0 : 1024)), "v"(l16 + 4096),
                    "s"(rown), "s"(L_OWN + (wv >> 1) * 8192 + (wv & 1) * 2048), "v"(own0), "v"(own0 ^ 64) : "memory");
    if (wv == 0) while (__builtin_amdgcn_s_memrealtime() - t_start < HOLD) __builtin_amdgcn_s_sleep(1);
    BAR();
    {
        u32x4 t[4], kt[2];
#pragma unroll
        for (int k = 0; k < 2; ++k) kt[k] = LDS(u32x4, ska + k * 1024);
#pragma unroll
        for (int k = 0; k < 3; ++k) t[k] = LDS(u32x4, sea + k * 1024);
        t[3] = LDS(u32x4, seb);
#pragma unroll
        for (int k = 0; k < 2; ++k) {
#pragma unroll
            for (int j = 0; j < 4; ++j) kt[k][j] |= j ? tg[j] : t0;
            ST16(kt[k], l16, so_ko, k * 1024);
        }
#pragma unroll
        for (int k = 0; k < 3; ++k) ST16(t[k], l16, so_eo, k * 1024);
#pragma unroll
        for (int j = 0; j < 4; ++j) t[3][j] = (t[3][j] & omask) | otag[j];
        ST16(t[3], oov, so_oo, 0);
    }
    __builtin_amdgcn_sched_barrier(0);
    u32x8 pbr;
    {
        const float sc = wv < 2 ? NLOG2E : VSCALE;
        unsigned wr[16];
        const unsigned blk = (wv & 3) * 8192 + (wv >> 2) * 4096, wb = L_RING + blk + h * 1024 + c * 4, wo = wfr + blk;
        const u32x4 pk4 = {PADKEY, PADKEY, PADKEY, PADKEY};
        LDS(u32x4, L_BIN + tid * 16) = pk4;
        LDS(u32x4, ska) = pk4;
        LDS(u32x4, ska + 1024) = pk4;
        LDS(u32x4, L_BIN + 24576 + (tid < 162 ? tid * 16 : BIG)) = pk4;
        asm volatile("s_waitcnt vmcnt(9)" ::: "memory");
#pragma unroll
        for (int j = 0; j < 16; ++j) wr[j] = LDS(unsigned, wb + ((j >> 3) * 16 + (j & 7)) * 128);
        asm volatile("s_waitcnt lgkmcnt(0)" ::: "memory");
        u32x4 f0[2], f1[2];
        if (wv < 4) {
#pragma unroll
            for (int k = 0; k < 2; ++k)
#pragma unroll
                for (int j = 0; j < 4; ++j) {
                    f0[k][j] = pack_bf2(lo_f(wr[8 * k + 2 * j]) * sc, lo_f(wr[8 * k + 2 * j + 1]) * sc);
                    f1[k][j] = pack_bf2(hi_f(wr[8 * k + 2 * j]) * sc, hi_f(wr[8 * k + 2 * j + 1]) * sc);
                }
        } else {
#pragma unroll
            for (int k = 0; k < 2; ++k)
#pragma unroll
                for (int j = 0; j < 4; ++j) {
                    f0[k][j] = __builtin_amdgcn_perm(wr[8 * k + 2 * j + 1], wr[8 * k + 2 * j], 0x05040100u);
                    f1[k][j] = __builtin_amdgcn_perm(wr[8 * k + 2 * j + 1], wr[8 * k + 2 * j], 0x07060302u);
                }
        }
#pragma unroll
        for (int k = 0; k < 2; ++k) {
            LDS(u32x4, wo + k * 1024) = f0[k];
            LDS(u32x4, wo + k * 1024 + 2048) = f1[k];
        }
    }
    u32x4 kq[2];
#define LOAD_KEYS(d0_, d1_, o0_, o1_) asm volatile("buffer_load_dwordx4 %0, %2, %4, %6 offen sc1\n\tbuffer_load_dwordx4 %1, %3, %4, %5 offen sc1" \
                 : "=&v"(d0_), "=&v"(d1_) : "v"(o0_), "v"(o1_), "s"(rws), "s"(so_k2), "s"(so_ki) : "memory")
    const unsigned fs = L_EI + EI_OVF + wv * 512;
    unsigned fsl = fs + (lane >> 3) * 8, flo = (lane >> 1) * 128 + (lane & 1) * 4;
#define FLAG_SAMPLE() asm volatile("s_mov_b32 m0, %1\n\ts_nop 0\n\tbuffer_load_dword %2, %0, %3 offen sc1 lds" :: "s"(rws), "s"(fs), "v"(flo), "s"(so_fl) : "memory")
    u32x4 ev4;
    unsigned long long pd0, pd1, pd2, pd3;
    auto try_e = [&]() {
        unsigned long long k0, k1, k2, k3;
#define E_STEP(K, P, EO, SO, IMM, LBL) "s_and_b64 exec, " K ", " P "\n\ts_cbranch_execz " LBL "f\n\ts_andn2_b64 " P ", " P ", exec\n\t" \
                                   "buffer_load_dwordx4 " EO ", %[rws], " SO " offen offset:" IMM " sc1 lds\n" LBL ":\n\t"
        asm volatile("ds_read_b64 v[244:245], %[a]\n\tds_read_b64 v[246:247], %[a] offset:64\n\tds_read_b64 v[248:249], %[a] offset:128\n\t"
                     "ds_read_b64 v[250:251], %[a] offset:192\n\ts_mov_b32 m0, %[dst]\n\ts_waitcnt lgkmcnt(0)\n\t"
                     "v_cmp_eq_u64 %[k0], %[ep], v[244:245]\n\tv_cmp_eq_u64 %[k1], %[ep], v[246:247]\n\t"
                     "v_cmp_eq_u64 %[k2], %[ep], v[248:249]\n\tv_cmp_eq_u64 %[k3], %[ep], v[250:251]\n\ts_nop 3\n\t"
                     E_STEP("%[k0]", "%[p0]", "%[e0]", "%[s0]", "0", "1") E_STEP("%[k1]", "%[p1]", "%[e1]", "%[s1]", "1024", "2")
                     E_STEP("%[k2]", "%[p2]", "%[e2]", "%[s2]", "2048", "3") E_STEP("%[k3]", "%[p3]", "%[e3]", "%[s3]", "3072", "4")
                     "s_mov_b64 exec, -1"
                     : [k0] "=&s"(k0), [k1] "=&s"(k1), [k2] "=&s"(k2), [k3] "=&s"(k3),
                       [p0] "+s"(pd0), [p1] "+s"(pd1), [p2] "+s"(pd2), [p3] "+s"(pd3)
                     : [a] "v"(fsl), [dst] "s"(L_EI + wv * 4096), [ep] "s"(next_epoch), [e0] "v"(ev4[0]), [e1] "v"(ev4[1]), [e2] "v"(ev4[2]), [e3] "v"(ev4[3]), [rws] "s"(rws), [s0] "s"(so_e4[0]),
                       [s1] "s"(so_e4[1]), [s2] "s"(so_e4[2]), [s3] "s"(so_e4[3])
                     : "memory", "scc", "v244", "v245", "v246", "v247", "v248", "v249", "v250", "v251");
    };
#define PENDING() ((pd0 | pd1 | pd2 | pd3) != 0)
    asm volatile("s_waitcnt vmcnt(6)" ::: "memory");
    BAR();
    u32x32 wg, wvv;
    auto read_frags = [&]() {
#pragma unroll
        for (int k = 0; k < 8; ++k) {
            const unsigned wp = wfr + ((k >> 1) & 1) * 8192 + (k >> 2) * 2048 + (k & 1) * 1024;
            u32x4 t0 = LDS(u32x4, wp), t1 = LDS(u32x4, wp + 16384);
#pragma unroll
            for (int j = 0; j < 4; ++j) { wg[4 * k + j] = t0[j]; wvv[4 * k + j] = t1[j]; }
        }
    };
    {
        f32x16 p = {0};
        bf16x8 fa[4], fb[4];
        unsigned cr[4];
#pragma unroll
        for (int j = 0; j < 4; ++j) cr[j] = LDS(unsigned, L_CW + h * 512 + c * 4 + j * 128);
#pragma unroll
        for (int k = 0; k < 4; ++k) { fa[k] = LDS(bf16x8, wfr + 4096 + (wv & 1) * 2048 + (k >> 1) * 8192 + (k & 1) * 1024); fb[k] = LDS(bf16x8, pa ^ (k * 32)); }
        __builtin_amdgcn_sched_barrier(0);
#pragma unroll
        for (int k = 0; k < 4; ++k) p = MFMA16(fa[k], fb[k], p);
        read_frags();
        __builtin_amdgcn_sched_barrier(0);
        pbr = u32x8{0, 0, pack_bf2(lo_f(cr[0]) * NLOG2E, lo_f(cr[1]) * NLOG2E), pack_bf2(lo_f(cr[2]) * NLOG2E, lo_f(cr[3]) * NLOG2E),
                    0, 0, pack_bf2(hi_f(cr[0]) * NLOG2E, hi_f(cr[1]) * NLOG2E), pack_bf2(hi_f(cr[2]) * NLOG2E, hi_f(cr[3]) * NLOG2E)};
        asm volatile("" : "+v"(pbr));
#pragma unroll
        for (int g = 0; g < 4; ++g)
            LDS(u32x2, L_PB + (pb ^ (g * 16))) = u32x2{pack_bf2(p[4 * g] * NLOG2E, p[4 * g + 1] * NLOG2E), pack_bf2(p[4 * g + 2] * NLOG2E, p[4 * g + 3] * NLOG2E)};
    }
    asm volatile("s_waitcnt vmcnt(0)" ::: "memory");
    BAR();
    if (wv == 0) ST16((u32x4{(unsigned)next_epoch, (unsigned)(next_epoch >> 32), 0, 0}), (lane >> 3) * FCH + (lane & 7) * 16, (unsigned)WS_FLG + w * 128, 0);
    LOAD_KEYS(kq[0], kq[1], ko, ko);
    f32x2 uu[7];
    {
        f32x16 u0 = {0}, u1 = {0};
        bf16x8 fa[4], fb[8];
#pragma unroll
        for (int k = 0; k < 4; ++k) {
            const unsigned fo = wfr + 4096 + 16384 + (k >> 1) * 8192 + (k & 1) * 1024;
            fa[k] = LDS(bf16x8, ua ^ (k * 32)); fb[2 * k] = LDS(bf16x8, fo); fb[2 * k + 1] = LDS(bf16x8, fo + 2048);
        }
        __builtin_amdgcn_sched_barrier(0);
#pragma unroll
        for (int k = 0; k < 4; ++k) { u0 = MFMA16(fa[k], fb[2 * k], u0); u1 = MFMA16(fa[k], fb[2 * k + 1], u1); }
#pragma unroll
        for (int i = 0; i < 7; ++i) {
            float m0 = u0[i] * NLOG2E, m1 = u1[i] * NLOG2E;
            asm volatile("" : "+v"(m0));
            uu[i] = f32x2{m0, m1};
            asm volatile("" : "+v"(uu[i]));
        }
    }
    asm volatile("s_waitcnt vmcnt(0)" : "+v"(kq[0]), "+v"(kq[1]) :: "memory");
#pragma unroll
    for (int i = 0; i < 2; ++i)
#pragma unroll
        for (int j = 0; j < 4; ++j) kq[i][j] ^= j ? tg[j] : tg0;
    if (__builtin_expect(__any(((kq[0][0] | kq[1][0]) & hm) | ((kq[0][1] | kq[0][2] | kq[0][3] | kq[1][1] | kq[1][2] | kq[1][3]) & TAGM)), 0)) {
#pragma unroll 1
        for (unsigned spins = 0; spins < (1u << 18); ++spins) {
            const unsigned b0 = (kq[0][0] & hm) | ((kq[0][1] | kq[0][2] | kq[0][3]) & TAGM), b1 = (kq[1][0] & hm) | ((kq[1][1] | kq[1][2] | kq[1][3]) & TAGM);
            if (!__any(b0 | b1)) break;
            __builtin_amdgcn_s_sleep(KSLEEP);
            u32x4 t0, t1;
            LOAD_KEYS(t0, t1, b0 ? ko : 0x40000000u, b1 ? ko : 0x40000000u);
            asm volatile("s_waitcnt vmcnt(0)" : "+v"(t0), "+v"(t1) :: "memory");
#pragma unroll
            for (int j = 0; j < 4; ++j) { kq[0][j] = b0 ? t0[j] ^ (j ? tg[j] : tg0) : kq[0][j]; kq[1][j] = b1 ? t1[j] ^ (j ? tg[j] : tg0) : kq[1][j]; }
        }
    }
    bool slow, early;
    unsigned oe0, oe1;
#define OVF_E() asm volatile("s_mov_b32 m0, %2\n\ts_nop 0\n\tbuffer_load_dword %3, %0, %1 offen sc1 lds\n\ts_add_u32 m0, m0, 256\n\ts_nop 0\n\t" \
                             "buffer_load_dword %4, %0, %1 offen sc1 lds" :: "s"(rws), "s"(so_ei), "s"(fs), "v"(oe0), "v"(oe1) : "memory", "scc")
    {
        const unsigned h0 = __builtin_amdgcn_update_dpp(0u, kq[0][0], 0x00, 0xf, 0xf, false);
        const unsigned h1 = __builtin_amdgcn_update_dpp(0u, kq[1][0], 0x00, 0xf, 0xf, false);
        const unsigned hs = j4 == 0 ? h0 : (j4 == 1 ? h1 : 0u);
        const unsigned ov = __builtin_elementwise_sub_sat(hs & 63u, 15u);
        const unsigned inc = wave_incl_scan(ov);
        const unsigned os = (hs >> 6) & 255u;
        const unsigned own = wave_incl_max(__builtin_amdgcn_ds_permute((ov > 0 ? inc - ov : 63u) * 4, ov > 0 ? lane * 4 : 0));
        unsigned tot = __builtin_amdgcn_readlane(inc, 63);
        slow = tot > 63 || __any(os + ov > 255 || ov == 48);
        tot = slow ? 0 : tot;
        unsigned oi = __builtin_amdgcn_ds_bpermute(own, of0 + os - (inc - ov)) + lane;
        oi = (unsigned)lane < tot ? oi : 0x8000000u;
        u32x4 ok4;
#define LOAD_OK() asm volatile("buffer_load_dwordx4 %0, %1, %2, %3 offen sc1" : "=v"(ok4) : "v"((oi & ~3u) * 4), "s"(rws), "s"(so_ok) : "memory")
        LOAD_OK();
        FLAG_SAMPLE();
        unsigned ps[8];
#pragma unroll
        for (int i = 0; i < 8; ++i) ps[i] = LADD(L_CNT + (((i & 3) || j4) ? kq[i >> 2][i & 3] >> 16 : hdm), 4u);
        unsigned oj = __builtin_amdgcn_ds_bpermute(own, of1 + ((hs >> 18) << 1) + ((hs >> 14) & 1) - (inc - ov)) + lane;
        u32x4 hh;
#pragma unroll
        for (int i = 0; i < 4; ++i) hh[i] = __builtin_amdgcn_ds_bpermute((lane >> 3) * 16 + (i & 1) * 128, i < 2 ? h0 : h1);
        const unsigned e0 = eb + ((h0 & 0x4000u) << 5), e1 = eb + ((h1 & 0x4000u) << 5) + (2048u << 16);
#pragma unroll
        for (int i = 0; i < 8; ++i)
            LDS(unsigned, L_BIN + ps[i]) = (kq[i >> 2][i & 3] & 0xffffu) + (i < 4 ? e0 : e1) + ((i & 3) << 19);
        oj = (unsigned)lane < tot ? oj : 0x8000000u;
        oe0 = __builtin_amdgcn_ds_bpermute((lane >> 1) * 4, oj) * 8 + (lane & 1) * 4;
        oe1 = __builtin_amdgcn_ds_bpermute((lane >> 1) * 4 + 128, oj) * 8 + (lane & 1) * 4;
#pragma unroll
        for (int i = 0; i < 4; ++i) ev4[i] = eo + ((hh[i] >> 18) << 4);
        pd0 = __ballot((unsigned)(lane & 7) * 2 <= (hh[0] & 63u)); pd1 = __ballot((unsigned)(lane & 7) * 2 <= (hh[1] & 63u));
        pd2 = __ballot((unsigned)(lane & 7) * 2 <= (hh[2] & 63u)); pd3 = __ballot((unsigned)(lane & 7) * 2 <= (hh[3] & 63u));
        asm volatile("s_waitcnt vmcnt(1)" : "+v"(ok4) :: "memory");
#pragma unroll 1
        for (unsigned spins = 0;; ++spins) {
#pragma unroll
            for (int j = 0; j < 4; ++j) ok4[j] ^= tg[j];
            if (__builtin_expect(!__any(oi < 0x8000000u && ((ok4[0] | ok4[1] | ok4[2] | ok4[3]) & TAGM)), 1) || spins > (1u << 18)) break;
            __builtin_amdgcn_s_sleep(KSLEEP);
            LOAD_OK();
            asm volatile("s_waitcnt vmcnt(0)" : "+v"(ok4) :: "memory");
        }
        const unsigned kk = (oi & 2) ? ((oi & 1) ? ok4[3] : ok4[2]) : ((oi & 1) ? ok4[1] : ok4[0]);
        const unsigned n8 = oi < 0x8000000u ? kk >> 16 : vbig;
        const unsigned p8 = LADD(L_CNT + n8, 4u);
        LDS(unsigned, L_BIN + (p8 | (n8 & vbig))) = (kk & 0xffffu) | ((unsigned)(EI_OVF + tid * 8) << 16);
        asm volatile("s_waitcnt vmcnt(0)" ::: "memory");
        early = __all(LDS(unsigned long long, fs + c8) == next_epoch);
        if (early) {
            asm volatile("s_mov_b32 m0, %1\n\ts_mov_b64 exec, %10\n\tbuffer_load_dwordx4 %2, %0, %6 offen sc1 lds\n\t"
                         "s_mov_b64 exec, %11\n\tbuffer_load_dwordx4 %3, %0, %7 offen offset:1024 sc1 lds\n\t"
                         "s_mov_b64 exec, %12\n\tbuffer_load_dwordx4 %4, %0, %8 offen offset:2048 sc1 lds\n\t"
                         "s_mov_b64 exec, %13\n\tbuffer_load_dwordx4 %5, %0, %9 offen offset:3072 sc1 lds\n\ts_mov_b64 exec, -1"
                         :: "s"(rws), "s"(L_EI + wv * 4096), "v"(ev4[0]), "v"(ev4[1]), "v"(ev4[2]), "v"(ev4[3]), "s"(so_e4[0]), "s"(so_e4[1]),
                            "s"(so_e4[2]), "s"(so_e4[3]), "s"(pd0), "s"(pd1), "s"(pd2), "s"(pd3) : "memory");
            OVF_E();
        } else {
            FLAG_SAMPLE();
        }
    }
    BAR();
    if (!early) {
        asm volatile("s_waitcnt vmcnt(0)" ::: "memory");
        try_e();
        early = !PENDING();
        if (early) OVF_E(); else FLAG_SAMPLE();
    }
    unsigned Q, T1, T2, T;
    u32x4 ra;
    unsigned sptr, kbase;
    unsigned long long ev0, ev1;
    const u32x4 srdi = {srd[0], srd[1] | (128u << 16), NN, 0x00020000u};
    {
        const u32x2 cc = LDS(u32x2, L_CNT + lane * 8);
        const unsigned a0 = lane < NPB / 2 ? max((cc[0] - bb + 12) >> 4, 1u) : 0u, a1 = lane < NPB / 2 ? max((cc[1] - bb - 260) >> 4, 1u) : 0u;
        const unsigned inc = wave_incl_scan(a0 + a1);
        const unsigned e1 = inc - a1, e0 = e1 - a0;
        Q = __builtin_amdgcn_readlane(inc, 63);
        const bool over = Q > QCAP || __any(max(a0, a1) > 17);
        Q = over ? 0 : Q;
        slow = slow || over;
        LDS(u32x2, L_QB + lane * 8) = u32x2{bb - e0 * 16, bb + 272 - e1 * 16};
        const unsigned S = (Q + 31) >> 5;
        T1 = (S + 1) >> 1; T2 = S >> 1; T = wv < 4 ? T1 : T2;
        const unsigned wbase = (wv & 3) * 8 * T + (wv < 4 ? 0 : 32 * T1);
        const unsigned sb0 = L_META + wv * 256;
        LDS(unsigned char, sb0 + min(e0 - wbase - 1, 128u)) = 1;
        LDS(unsigned char, sb0 + min(e1 - wbase - 1, 128u)) = 1;
        const unsigned long long k0 = __ballot(LDS(unsigned char, scr)), k1 = __ballot(LDS(unsigned char, scr + 64));
        const unsigned nb = min((unsigned)(__popcll(__ballot(e0 <= wbase)) + __popcll(__ballot(e1 <= wbase))) - 1, (unsigned)NPB);
        unsigned nd[2];
        nd[0] = __builtin_amdgcn_mbcnt_hi((unsigned)(k0 >> 32), __builtin_amdgcn_mbcnt_lo((unsigned)k0, nb));
        nd[1] = __builtin_amdgcn_mbcnt_hi((unsigned)(k1 >> 32), __builtin_amdgcn_mbcnt_lo((unsigned)k1, nb + __popcll(k0)));
        unsigned qb[2];
#pragma unroll
        for (int g = 0; g < 2; ++g) { qb[g] = LDS(unsigned, L_QB + nd[g] * 4); LDS(unsigned char, scr + 64 * g) = nd[g]; }
        const unsigned kl = sb0 + __umul24(lane & 7, T) + (lane >> 3) * 2;
        unsigned nn[3];
#pragma unroll
        for (int it = 0; it < 3; ++it) nn[it] = LDS(unsigned char, kl + it);
#pragma unroll
        for (int X = 0; X < 4; ++X) ra[X] = LDS(unsigned char, sb0 + (4 * h + X) * T);
        u32x4 t[2];
#pragma unroll
        for (int g = 0; g < 2; ++g) t[g] = LDS(u32x4, L_BIN + g * 1024 + (qb[g] + wbase * 16 + l16));
        kbase = L_KEY + wbase * 16;
        sptr = kbase + __umul24(ks, T);
#pragma unroll
        for (int g = 0; g < 2; ++g) LDS(u32x4, (lane + 64 * g < 8 * T ? L_KEY + wbase * 16 + l16 : BIG) + g * 1024) = t[g];
        asm volatile("ds_read_u16 v224, %1\n\tds_read_u16 v226, %1 offset:4\n\tds_read_u16 v228, %1 offset:8\n\tds_read_u16 v230, %1 offset:12\n\t"
                     "s_mov_b32 m0, %3\n\ts_waitcnt lgkmcnt(0)\n\t"
                     "buffer_load_dwordx4 v[224:225], %2, 0 idxen offen lds\n\ts_add_u32 m0, m0, 1024\n\tds_read_u16 v224, %1 offset:16\n\t"
                     "buffer_load_dwordx4 v[226:227], %2, 0 idxen offen lds\n\ts_add_u32 m0, m0, 1024\n\tds_read_u16 v226, %1 offset:20\n\t"
                     "buffer_load_dwordx4 v[228:229], %2, 0 idxen offen lds\n\ts_add_u32 m0, m0, 1024\n\tds_read_u16 v228, %1 offset:24\n\t"
                     "buffer_load_dwordx4 v[230:231], %2, 0 idxen offen lds\n\tds_read_u16 v230, %1 offset:28\n\ts_waitcnt lgkmcnt(0)"
                     : "+{v[224:231]}"(dpr) : "v"(sptr), "s"(srdi), "s"(ring) : "memory", "scc");
#pragma unroll
        for (int it = 0; it < 2; ++it) LDS(unsigned short, pmw + it * 16) = nn[it] * 128 + (nn[it] & 7) * 8;
        ev0 = __ballot(nn[0] != nn[1]);
        ev1 = __ballot(nn[1] != nn[2]);
#pragma unroll
        for (int X = 0; X < 4; ++X) ra[X] = ra[X] * 256 + c8;
        const unsigned n0 = nn[1], n1 = (T & 1) ? nn[0] : nn[2];
        const bool tl = (unsigned)(lane >> 3) == ((T - 1) >> 1) && n0 == n1 && n0 < NPB;
        const unsigned old = __hip_atomic_fetch_max((LAS unsigned*)(tl ? L_TP + n0 * 4 : BIG), ring + (lane & 7) * 1024, __ATOMIC_RELAXED, __HIP_MEMORY_SCOPE_WORKGROUP);
        LDS(unsigned, old > L_ZR ? L_MULTI : BIG) = 1;
    }
    if (!early) {
        asm volatile("s_waitcnt vmcnt(4)" ::: "memory");
        try_e();
        if (PENDING()) FLAG_SAMPLE();
    }
    const int rho = (c & 1) | ((c >> 1) & 2) | ((c >> 2) & 4) | ((c & 2) << 2) | ((c & 8) << 1);
    unsigned afr = rho * 128 + ((h ^ ((c >> 1) & 7)) << 4);
    u32x4 aer;
    {
        const unsigned oh = (((c >> 2) & 1) == h) ? (0x3F80u << (16 * (c & 1))) : 0u;
        aer = u32x4{c < 16 ? oh : 0u, c < 16 ? 0u : oh, 0, 0};
    }
    u32x2 msd = {c8, (unsigned)(((lane >> 4) & 1) * 32 + (lane & 3) * 8)};
    u32x8 msc;
    {
        const unsigned aoff0 = ring + afr;
        msc = u32x8{aoff0, aoff0 ^ 32, aoff0 ^ 64, aoff0 ^ 96,
                    kbase + 2 + (((c >> 1) & 1) | ((c >> 2) & 2)) * 4 + __umul24(((c & 5) | ((c >> 3) & 2)) * 16, T),
                    sptr, (unsigned)(L_META + wv * 256 + h * 8 + ((lane >> 2) & 3) * 2), 0};
    }
    asm volatile("" : "+v"(aer), "+v"(msd), "+v"(msc));
    unsigned ga = L_AGG + (wv * 14 + h * 7) * 256 + c8, ob = L_EI + wv * 2048 + h * 896 + c * 4, os = L_EI + wv * 2048 + l16;
    unsigned o16 = (unsigned)(b * NPB + wv * 14) * 128 + l16, ta = ring + h * 4096 + c8, tpa = L_TP + (wv * 14 + h * 7) * 4;
    unsigned o17 = lane < 48 ? o16 + 1024 : 0x40000000u;
    asm volatile("" : "+v"(ga), "+v"(ob), "+v"(os), "+v"(o16), "+v"(o17), "+v"(ta), "+v"(tpa));
    if (early) {
        asm volatile("s_waitcnt vmcnt(4)" ::: "memory");
    } else {
#pragma unroll 1
        for (unsigned spins = 0; PENDING() && spins < (1u << 18); ++spins) {
            asm volatile("s_waitcnt vmcnt(0)" ::: "memory");
            try_e();
            if (!PENDING()) break;
            __builtin_amdgcn_s_sleep(PSLEEP);
            FLAG_SAMPLE();
        }
        OVF_E();
        asm volatile("s_waitcnt vmcnt(0)" ::: "memory");
    }
    asm volatile("s_add_u32 m0, %2, 4096\n\ts_nop 0\n\t"
                 "buffer_load_dwordx4 v[224:225], %1, 0 idxen offen lds\n\ts_add_u32 m0, m0, 1024\n\ts_nop 0\n\t"
                 "buffer_load_dwordx4 v[226:227], %1, 0 idxen offen lds\n\ts_add_u32 m0, m0, 1024\n\ts_nop 0\n\t"
                 "buffer_load_dwordx4 v[228:229], %1, 0 idxen offen lds\n\ts_add_u32 m0, m0, 1024\n\ts_nop 0\n\t"
                 "buffer_load_dwordx4 v[230:231], %1, 0 idxen offen lds"
                 : "+{v[224:231]}"(dpr) : "s"(srdi), "s"(ring) : "memory", "scc");
    if (T > 0) {
        unsigned si, stmp;
        u32x8 acc;
        asm volatile(
    "s_mov_b32 s40, %[srd0]\n\t"
    "s_mov_b32 s41, %[srd1]\n\t"
    "s_mov_b32 s43, 0x00020000\n\t"
    "s_mov_b32 %[i], 0\n\t"
    "ds_read_u16 v242, v238 offset:0\n\t"
    "ds_read_u16 v239, v236 offset:0\n\t"
    "ds_read_u16 v224, v237 offset:32\n\t"
    "ds_read_u16 v226, v237 offset:36\n\t"
    "ds_read_u16 v228, v237 offset:40\n\t"
    "ds_read_u16 v230, v237 offset:44\n\t"
    "s_lshl_b64 s[44:45], %[ev0], 8\n\t"
    "s_lshl_b64 s[46:47], %[ev1], 8\n\t"
    "s_waitcnt lgkmcnt(4)\n\t"
    "v_xor_b32 v242, v242, v241\n\t"
    "ds_read_b64_tr_b16 v[212:213], v242 offset:28544\n\t"
    "ds_read_b64_tr_b16 v[216:217], v242 offset:28608\n\t"
    "v_mov_b64 v[244:245], 0\n\t"
    "v_mov_b64 v[246:247], 0\n\t"
    "v_mov_b64 v[248:249], 0\n\t"
    "v_mov_b64 v[250:251], 0\n\t"
    "v_mov_b64 v[134:135], 0\n\t"
    "v_mov_b32 v132, 1.0\n\t"
    "v_mov_b32 v133, 1.0\n\t"
    "v_mov_b64 v[142:143], 0\n\t"
    "v_mov_b32 v140, 1.0\n\t"
    "v_mov_b32 v141, 1.0\n\t"
    "v_mov_b64 v[138:139], 0\n\t"
    "v_mov_b32 v136, 1.0\n\t"
    "v_mov_b32 v137, 1.0\n\t"
    "v_mov_b64 v[146:147], 0\n\t"
    "v_mov_b32 v144, 1.0\n\t"
    "v_mov_b32 v145, 1.0\n\t"
    "s_waitcnt vmcnt(4)\n\t"
    "ds_read_b128 v[180:183], v232 offset:0\n\t"
    "ds_read_b128 v[184:187], v233 offset:0\n\t"
    "ds_read_b128 v[188:191], v234 offset:0\n\t"
    "ds_read_b128 v[192:195], v235 offset:0\n\t"
    "s_waitcnt lgkmcnt(0)\n\t"
    "s_barrier\n\t"
    "ds_read_b64 v[222:223], v239 offset:43264\n\t"
    "s_waitcnt lgkmcnt(0)\n\t"
    "s_branch 6f\n\t"
    ".p2align 6\n\t"
    "s_nop 0\n\t"
    "s_nop 0\n\t"
    "1:\n\t"
    "ds_read_b128 v[180:183], v232 offset:0\n\t"
    "ds_read_b128 v[184:187], v233 offset:0\n\t"
    "ds_read_b128 v[188:191], v234 offset:0\n\t"
    "ds_read_b128 v[192:195], v235 offset:0\n\t"
    "s_and_b32 %[tmp], s46, 17\n\t"
    "s_cbranch_scc1 20f\n\t"
    "24:\n\t"
    "s_and_b32 %[tmp], s46, 34\n\t"
    "s_cbranch_scc1 21f\n\t"
    "25:\n\t"
    "s_and_b32 %[tmp], s46, 68\n\t"
    "s_cbranch_scc1 22f\n\t"
    "26:\n\t"
    "s_and_b32 %[tmp], s46, 136\n\t"
    "s_cbranch_scc1 23f\n\t"
    "27:\n\t"
    "s_lshr_b64 s[46:47], s[46:47], 8\n\t"
    "s_cmp_ge_u32 %[i], %[T]\n\t"
    "s_setprio 0\n\t"
    "v_add_u32 v236, 32, v236\n\t"
    "v_add_u32 v237, 32, v237\n\t"
    "v_add_u32 v238, 32, v238\n\t"
    "v_rcp_f32 v132, v132\n\t"
    "v_rcp_f32 v133, v133\n\t"
    "v_rcp_f32 v140, v140\n\t"
    "v_rcp_f32 v141, v141\n\t"
    "v_rcp_f32 v136, v136\n\t"
    "v_rcp_f32 v137, v137\n\t"
    "v_rcp_f32 v144, v144\n\t"
    "v_rcp_f32 v145, v145\n\t"
    "v_fmac_f32 v244, v134, v132\n\t"
    "v_fmac_f32 v246, v135, v133\n\t"
    "v_fmac_f32 v248, v142, v140\n\t"
    "v_fmac_f32 v250, v143, v141\n\t"
    "v_fmac_f32 v245, v138, v136\n\t"
    "v_fmac_f32 v247, v139, v137\n\t"
    "v_fmac_f32 v249, v146, v144\n\t"
    "v_fmac_f32 v251, v147, v145\n\t"
    "v_exp_f32 v84, v84\n\t"
    "v_exp_f32 v85, v85\n\t"
    "v_exp_f32 v86, v86\n\t"
    "v_exp_f32 v87, v87\n\t"
    "v_exp_f32 v88, v88\n\t"
    "v_exp_f32 v89, v89\n\t"
    "v_exp_f32 v90, v90\n\t"
    "v_exp_f32 v91, v91\n\t"
    "v_add_f32 v84, 1.0, v84\n\t"
    "v_add_f32 v85, 1.0, v85\n\t"
    "v_fma_f32 v116, v84, v86, v84\n\t"
    "v_fma_f32 v117, v85, v87, v85\n\t"
    "v_exp_f32 v92, v92\n\t"
    "v_exp_f32 v93, v93\n\t"
    "v_exp_f32 v94, v94\n\t"
    "v_exp_f32 v95, v95\n\t"
    "v_exp_f32 v96, v96\n\t"
    "v_exp_f32 v97, v97\n\t"
    "v_exp_f32 v98, v98\n\t"
    "v_exp_f32 v99, v99\n\t"
    "v_add_f32 v92, 1.0, v92\n\t"
    "v_add_f32 v93, 1.0, v93\n\t"
    "v_fma_f32 v118, v92, v94, v92\n\t"
    "v_fma_f32 v119, v93, v95, v93\n\t"
    "v_exp_f32 v100, v100\n\t"
    "v_exp_f32 v101, v101\n\t"
    "v_exp_f32 v102, v102\n\t"
    "v_exp_f32 v103, v103\n\t"
    "v_exp_f32 v104, v104\n\t"
    "v_exp_f32 v105, v105\n\t"
    "v_exp_f32 v106, v106\n\t"
    "v_exp_f32 v107, v107\n\t"
    "v_add_f32 v100, 1.0, v100\n\t"
    "v_add_f32 v101, 1.0, v101\n\t"
    "v_exp_f32 v108, v108\n\t"
    "v_exp_f32 v109, v109\n\t"
    "v_exp_f32 v110, v110\n\t"
    "v_exp_f32 v111, v111\n\t"
    "v_exp_f32 v112, v112\n\t"
    "v_exp_f32 v113, v113\n\t"
    "s_waitcnt lgkmcnt(0)\n\t"
    "s_setprio 2\n\t"
    "v_exp_f32 v114, v114\n\t"
    "v_exp_f32 v115, v115\n\t"
    "v_add_f32 v108, 1.0, v108\n\t"
    "v_add_f32 v109, 1.0, v109\n\t"
    "v_pk_fma_f32 v[148:149], v[148:149], v[86:87], v[148:149]\n\t"
    "v_pk_fma_f32 v[156:157], v[156:157], v[94:95], v[156:157]\n\t"
    "v_pk_fma_f32 v[164:165], v[164:165], v[102:103], v[164:165]\n\t"
    "v_pk_fma_f32 v[172:173], v[172:173], v[110:111], v[172:173]\n\t"
    "v_pk_fma_f32 v[148:149], v[150:151], v[84:85], v[148:149]\n\t"
    "v_pk_fma_f32 v[156:157], v[158:159], v[92:93], v[156:157]\n\t"
    "v_pk_fma_f32 v[164:165], v[166:167], v[100:101], v[164:165]\n\t"
    "v_pk_fma_f32 v[172:173], v[174:175], v[108:109], v[172:173]\n\t"
    "v_pk_fma_f32 v[148:149], v[148:149], v[88:89], v[148:149]\n\t"
    "v_pk_fma_f32 v[156:157], v[156:157], v[96:97], v[156:157]\n\t"
    "v_pk_fma_f32 v[100:101], v[100:101], v[102:103], v[100:101]\n\t"
    "v_pk_fma_f32 v[108:109], v[108:109], v[110:111], v[108:109]\n\t"
    "v_pk_fma_f32 v[148:149], v[152:153], v[116:117], v[148:149]\n\t"
    "v_pk_fma_f32 v[156:157], v[160:161], v[118:119], v[156:157]\n\t"
    "v_pk_fma_f32 v[164:165], v[164:165], v[104:105], v[164:165]\n\t"
    "v_pk_fma_f32 v[172:173], v[172:173], v[112:113], v[172:173]\n\t"
    "v_pk_fma_f32 v[116:117], v[116:117], v[88:89], v[116:117]\n\t"
    "v_pk_fma_f32 v[118:119], v[118:119], v[96:97], v[118:119]\n\t"
    "v_pk_fma_f32 v[164:165], v[168:169], v[100:101], v[164:165]\n\t"
    "v_pk_fma_f32 v[172:173], v[176:177], v[108:109], v[172:173]\n\t"
    "v_pk_fma_f32 v[148:149], v[148:149], v[90:91], v[148:149]\n\t"
    "v_pk_fma_f32 v[156:157], v[156:157], v[98:99], v[156:157]\n\t"
    "v_pk_fma_f32 v[100:101], v[100:101], v[104:105], v[100:101]\n\t"
    "v_pk_fma_f32 v[108:109], v[108:109], v[112:113], v[108:109]\n\t"
    "v_pk_fma_f32 v[134:135], v[154:155], v[116:117], v[148:149]\n\t"
    "v_pk_fma_f32 v[142:143], v[162:163], v[118:119], v[156:157]\n\t"
    "v_pk_fma_f32 v[164:165], v[164:165], v[106:107], v[164:165]\n\t"
    "v_pk_fma_f32 v[172:173], v[172:173], v[114:115], v[172:173]\n\t"
    "v_pk_fma_f32 v[132:133], v[116:117], v[90:91], v[116:117]\n\t"
    "v_pk_fma_f32 v[140:141], v[118:119], v[98:99], v[118:119]\n\t"
    "v_pk_fma_f32 v[138:139], v[170:171], v[100:101], v[164:165]\n\t"
    "v_pk_fma_f32 v[146:147], v[178:179], v[108:109], v[172:173]\n\t"
    "v_pk_fma_f32 v[136:137], v[100:101], v[106:107], v[100:101]\n\t"
    "v_pk_fma_f32 v[144:145], v[108:109], v[114:115], v[108:109]\n\t"
    "s_cbranch_scc1 4f\n\t"
    "6:\n\t"
    "v_mfma_f32_32x32x16_bf16 v[84:99], v[220:223], v[212:215], 0\n\t"
    "s_setprio 1\n\t"
    "s_add_u32 %[tmp], %[i], 2\n\t"
    "s_cmp_lt_u32 %[tmp], %[T]\n\t"
    "s_cselect_b32 s42, %[nrec], 0\n\t"
    "s_add_u32 m0, %[ring], 0\n\t"
    "v_mfma_f32_32x32x16_bf16 v[84:99], v[180:183], v[20:23], v[84:99]\n\t"
    "buffer_load_dwordx4 v[224:225], s[40:43], 0 idxen offen  lds\n\t"
    "s_add_u32 m0, m0, 1024\n\t"
    "v_mfma_f32_32x32x16_bf16 v[84:99], v[184:187], v[24:27], v[84:99]\n\t"
    "buffer_load_dwordx4 v[226:227], s[40:43], 0 idxen offen  lds\n\t"
    "s_add_u32 m0, m0, 1024\n\t"
    "v_mfma_f32_32x32x16_bf16 v[84:99], v[188:191], v[28:31], v[84:99]\n\t"
    "buffer_load_dwordx4 v[228:229], s[40:43], 0 idxen offen  lds\n\t"
    "s_add_u32 m0, m0, 1024\n\t"
    "v_mfma_f32_32x32x16_bf16 v[84:99], v[192:195], v[32:35], v[84:99]\n\t"
    "buffer_load_dwordx4 v[230:231], s[40:43], 0 idxen offen  lds\n\t"
    "v_mfma_f32_32x32x16_bf16 v[100:115], v[220:223], v[216:219], 0\n\t"
    "ds_read_u16 v242, v238 offset:16\n\t"
    "v_mfma_f32_32x32x16_bf16 v[100:115], v[180:183], v[36:39], v[100:115]\n\t"
    "ds_read_u16 v239, v236 offset:16\n\t"
    "v_mfma_f32_32x32x16_bf16 v[100:115], v[184:187], v[40:43], v[100:115]\n\t"
    "ds_read_u16 v224, v237 offset:48\n\t"
    "v_mfma_f32_32x32x16_bf16 v[100:115], v[188:191], v[44:47], v[100:115]\n\t"
    "ds_read_u16 v226, v237 offset:52\n\t"
    "v_mfma_f32_32x32x16_bf16 v[100:115], v[192:195], v[48:51], v[100:115]\n\t"
    "ds_read_u16 v228, v237 offset:56\n\t"
    "v_mfma_f32_32x32x16_bf16 v[148:163], v[180:183], v[52:55], 0\n\t"
    "ds_read_u16 v230, v237 offset:60\n\t"
    "v_mfma_f32_32x32x16_bf16 v[148:163], v[184:187], v[56:59], v[148:163]\n\t"
    "s_waitcnt lgkmcnt(4)\n\t"
    "v_xor_b32 v242, v242, v241\n\t"
    "v_mfma_f32_32x32x16_bf16 v[148:163], v[188:191], v[60:63], v[148:163]\n\t"
    "ds_read_b64 v[222:223], v239 offset:43264\n\t"
    "v_mfma_f32_32x32x16_bf16 v[148:163], v[192:195], v[64:67], v[148:163]\n\t"
    "ds_read_b64_tr_b16 v[212:213], v242 offset:28544\n\t"
    "v_mfma_f32_32x32x16_bf16 v[164:179], v[180:183], v[68:71], 0\n\t"
    "ds_read_b64_tr_b16 v[216:217], v242 offset:28608\n\t"
    "v_mfma_f32_32x32x16_bf16 v[164:179], v[184:187], v[72:75], v[164:179]\n\t"
    "s_add_u32 %[i], %[i], 1\n\t"
    "v_mfma_f32_32x32x16_bf16 v[164:179], v[188:191], v[76:79], v[164:179]\n\t"
    "s_setprio 3\n\t"
    "s_waitcnt vmcnt(4)\n\t"
    "v_mfma_f32_32x32x16_bf16 v[164:179], v[192:195], v[80:83], v[164:179]\n\t"
    "ds_read_b128 v[180:183], v232 offset:4096\n\t"
    "ds_read_b128 v[184:187], v233 offset:4096\n\t"
    "ds_read_b128 v[188:191], v234 offset:4096\n\t"
    "ds_read_b128 v[192:195], v235 offset:4096\n\t"
    "s_and_b32 %[tmp], s44, 17\n\t"
    "s_cbranch_scc1 30f\n\t"
    "34:\n\t"
    "s_and_b32 %[tmp], s44, 34\n\t"
    "s_cbranch_scc1 31f\n\t"
    "35:\n\t"
    "s_and_b32 %[tmp], s44, 68\n\t"
    "s_cbranch_scc1 32f\n\t"
    "36:\n\t"
    "s_and_b32 %[tmp], s44, 136\n\t"
    "s_cbranch_scc1 33f\n\t"
    "37:\n\t"
    "s_lshr_b64 s[44:45], s[44:45], 8\n\t"
    "s_cmp_ge_u32 %[i], %[T]\n\t"
    "s_setprio 0\n\t"
    "v_rcp_f32 v132, v132\n\t"
    "v_rcp_f32 v133, v133\n\t"
    "v_rcp_f32 v140, v140\n\t"
    "v_rcp_f32 v141, v141\n\t"
    "v_rcp_f32 v136, v136\n\t"
    "v_rcp_f32 v137, v137\n\t"
    "v_rcp_f32 v144, v144\n\t"
    "v_rcp_f32 v145, v145\n\t"
    "v_fmac_f32 v244, v134, v132\n\t"
    "v_fmac_f32 v246, v135, v133\n\t"
    "v_fmac_f32 v248, v142, v140\n\t"
    "v_fmac_f32 v250, v143, v141\n\t"
    "v_fmac_f32 v245, v138, v136\n\t"
    "v_fmac_f32 v247, v139, v137\n\t"
    "v_fmac_f32 v249, v146, v144\n\t"
    "v_fmac_f32 v251, v147, v145\n\t"
    "v_exp_f32 v84, v84\n\t"
    "v_exp_f32 v85, v85\n\t"
    "v_exp_f32 v86, v86\n\t"
    "v_exp_f32 v87, v87\n\t"
    "v_exp_f32 v88, v88\n\t"
    "v_exp_f32 v89, v89\n\t"
    "v_exp_f32 v90, v90\n\t"
    "v_exp_f32 v91, v91\n\t"
    "v_add_f32 v84, 1.0, v84\n\t"
    "v_add_f32 v85, 1.0, v85\n\t"
    "v_fma_f32 v116, v84, v86, v84\n\t"
    "v_fma_f32 v117, v85, v87, v85\n\t"
    "v_exp_f32 v92, v92\n\t"
    "v_exp_f32 v93, v93\n\t"
    "v_exp_f32 v94, v94\n\t"
    "v_exp_f32 v95, v95\n\t"
    "v_exp_f32 v96, v96\n\t"
    "v_exp_f32 v97, v97\n\t"
    "v_exp_f32 v98, v98\n\t"
    "v_exp_f32 v99, v99\n\t"
    "v_add_f32 v92, 1.0, v92\n\t"
    "v_add_f32 v93, 1.0, v93\n\t"
    "v_fma_f32 v118, v92, v94, v92\n\t"
    "v_fma_f32 v119, v93, v95, v93\n\t"
    "v_exp_f32 v100, v100\n\t"
    "v_exp_f32 v101, v101\n\t"
    "v_exp_f32 v102, v102\n\t"
    "v_exp_f32 v103, v103\n\t"
    "v_exp_f32 v104, v104\n\t"
    "v_exp_f32 v105, v105\n\t"
    "v_exp_f32 v106, v106\n\t"
    "v_exp_f32 v107, v107\n\t"
    "v_add_f32 v100, 1.0, v100\n\t"
    "v_add_f32 v101, 1.0, v101\n\t"
    "v_exp_f32 v108, v108\n\t"
    "v_exp_f32 v109, v109\n\t"
    "v_exp_f32 v110, v110\n\t"
    "v_exp_f32 v111, v111\n\t"
    "v_exp_f32 v112, v112\n\t"
    "v_exp_f32 v113, v113\n\t"
    "s_waitcnt lgkmcnt(0)\n\t"
    "s_setprio 2\n\t"
    "v_exp_f32 v114, v114\n\t"
    "v_exp_f32 v115, v115\n\t"
    "v_add_f32 v108, 1.0, v108\n\t"
    "v_add_f32 v109, 1.0, v109\n\t"
    "v_pk_fma_f32 v[148:149], v[148:149], v[86:87], v[148:149]\n\t"
    "v_pk_fma_f32 v[156:157], v[156:157], v[94:95], v[156:157]\n\t"
    "v_pk_fma_f32 v[164:165], v[164:165], v[102:103], v[164:165]\n\t"
    "v_pk_fma_f32 v[172:173], v[172:173], v[110:111], v[172:173]\n\t"
    "v_pk_fma_f32 v[148:149], v[150:151], v[84:85], v[148:149]\n\t"
    "v_pk_fma_f32 v[156:157], v[158:159], v[92:93], v[156:157]\n\t"
    "v_pk_fma_f32 v[164:165], v[166:167], v[100:101], v[164:165]\n\t"
    "v_pk_fma_f32 v[172:173], v[174:175], v[108:109], v[172:173]\n\t"
    "v_pk_fma_f32 v[148:149], v[148:149], v[88:89], v[148:149]\n\t"
    "v_pk_fma_f32 v[156:157], v[156:157], v[96:97], v[156:157]\n\t"
    "v_pk_fma_f32 v[100:101], v[100:101], v[102:103], v[100:101]\n\t"
    "v_pk_fma_f32 v[108:109], v[108:109], v[110:111], v[108:109]\n\t"
    "v_pk_fma_f32 v[148:149], v[152:153], v[116:117], v[148:149]\n\t"
    "v_pk_fma_f32 v[156:157], v[160:161], v[118:119], v[156:157]\n\t"
    "v_pk_fma_f32 v[164:165], v[164:165], v[104:105], v[164:165]\n\t"
    "v_pk_fma_f32 v[172:173], v[172:173], v[112:113], v[172:173]\n\t"
    "v_pk_fma_f32 v[116:117], v[116:117], v[88:89], v[116:117]\n\t"
    "v_pk_fma_f32 v[118:119], v[118:119], v[96:97], v[118:119]\n\t"
    "v_pk_fma_f32 v[164:165], v[168:169], v[100:101], v[164:165]\n\t"
    "v_pk_fma_f32 v[172:173], v[176:177], v[108:109], v[172:173]\n\t"
    "v_pk_fma_f32 v[148:149], v[148:149], v[90:91], v[148:149]\n\t"
    "v_pk_fma_f32 v[156:157], v[156:157], v[98:99], v[156:157]\n\t"
    "v_pk_fma_f32 v[100:101], v[100:101], v[104:105], v[100:101]\n\t"
    "v_pk_fma_f32 v[108:109], v[108:109], v[112:113], v[108:109]\n\t"
    "v_pk_fma_f32 v[134:135], v[154:155], v[116:117], v[148:149]\n\t"
    "v_pk_fma_f32 v[142:143], v[162:163], v[118:119], v[156:157]\n\t"
    "v_pk_fma_f32 v[164:165], v[164:165], v[106:107], v[164:165]\n\t"
    "v_pk_fma_f32 v[172:173], v[172:173], v[114:115], v[172:173]\n\t"
    "v_pk_fma_f32 v[132:133], v[116:117], v[90:91], v[116:117]\n\t"
    "v_pk_fma_f32 v[140:141], v[118:119], v[98:99], v[118:119]\n\t"
    "v_pk_fma_f32 v[138:139], v[170:171], v[100:101], v[164:165]\n\t"
    "v_pk_fma_f32 v[146:147], v[178:179], v[108:109], v[172:173]\n\t"
    "v_pk_fma_f32 v[136:137], v[100:101], v[106:107], v[100:101]\n\t"
    "v_pk_fma_f32 v[144:145], v[108:109], v[114:115], v[108:109]\n\t"
    "s_cbranch_scc1 9f\n\t"
    "v_mfma_f32_32x32x16_bf16 v[84:99], v[220:223], v[212:215], 0\n\t"
    "s_setprio 1\n\t"
    "s_add_u32 %[tmp], %[i], 2\n\t"
    "s_cmp_lt_u32 %[tmp], %[T]\n\t"
    "s_cselect_b32 s42, %[nrec], 0\n\t"
    "s_add_u32 m0, %[ring], 4096\n\t"
    "v_mfma_f32_32x32x16_bf16 v[84:99], v[180:183], v[20:23], v[84:99]\n\t"
    "buffer_load_dwordx4 v[224:225], s[40:43], 0 idxen offen  lds\n\t"
    "s_add_u32 m0, m0, 1024\n\t"
    "v_mfma_f32_32x32x16_bf16 v[84:99], v[184:187], v[24:27], v[84:99]\n\t"
    "buffer_load_dwordx4 v[226:227], s[40:43], 0 idxen offen  lds\n\t"
    "s_add_u32 m0, m0, 1024\n\t"
    "v_mfma_f32_32x32x16_bf16 v[84:99], v[188:191], v[28:31], v[84:99]\n\t"
    "buffer_load_dwordx4 v[228:229], s[40:43], 0 idxen offen  lds\n\t"
    "s_add_u32 m0, m0, 1024\n\t"
    "v_mfma_f32_32x32x16_bf16 v[84:99], v[192:195], v[32:35], v[84:99]\n\t"
    "buffer_load_dwordx4 v[230:231], s[40:43], 0 idxen offen  lds\n\t"
    "v_mfma_f32_32x32x16_bf16 v[100:115], v[220:223], v[216:219], 0\n\t"
    "ds_read_u16 v242, v238 offset:32\n\t"
    "v_mfma_f32_32x32x16_bf16 v[100:115], v[180:183], v[36:39], v[100:115]\n\t"
    "ds_read_u16 v239, v236 offset:32\n\t"
    "v_mfma_f32_32x32x16_bf16 v[100:115], v[184:187], v[40:43], v[100:115]\n\t"
    "ds_read_u16 v224, v237 offset:64\n\t"
    "v_mfma_f32_32x32x16_bf16 v[100:115], v[188:191], v[44:47], v[100:115]\n\t"
    "ds_read_u16 v226, v237 offset:68\n\t"
    "v_mfma_f32_32x32x16_bf16 v[100:115], v[192:195], v[48:51], v[100:115]\n\t"
    "ds_read_u16 v228, v237 offset:72\n\t"
    "v_mfma_f32_32x32x16_bf16 v[148:163], v[180:183], v[52:55], 0\n\t"
    "ds_read_u16 v230, v237 offset:76\n\t"
    "v_mfma_f32_32x32x16_bf16 v[148:163], v[184:187], v[56:59], v[148:163]\n\t"
    "s_waitcnt lgkmcnt(4)\n\t"
    "v_xor_b32 v242, v242, v241\n\t"
    "v_mfma_f32_32x32x16_bf16 v[148:163], v[188:191], v[60:63], v[148:163]\n\t"
    "ds_read_b64 v[222:223], v239 offset:43264\n\t"
    "v_mfma_f32_32x32x16_bf16 v[148:163], v[192:195], v[64:67], v[148:163]\n\t"
    "ds_read_b64_tr_b16 v[212:213], v242 offset:28544\n\t"
    "v_mfma_f32_32x32x16_bf16 v[164:179], v[180:183], v[68:71], 0\n\t"
    "ds_read_b64_tr_b16 v[216:217], v242 offset:28608\n\t"
    "v_mfma_f32_32x32x16_bf16 v[164:179], v[184:187], v[72:75], v[164:179]\n\t"
    "s_add_u32 %[i], %[i], 1\n\t"
    "v_mfma_f32_32x32x16_bf16 v[164:179], v[188:191], v[76:79], v[164:179]\n\t"
    "s_setprio 3\n\t"
    "s_waitcnt vmcnt(4)\n\t"
    "v_mfma_f32_32x32x16_bf16 v[164:179], v[192:195], v[80:83], v[164:179]\n\t"
    "s_branch 1b\n\t"
    "20:\n\t"
    "s_bitcmp1_b32 s46, 0\n\t"
    "s_cselect_b32 exec_lo, -1, 0\n\t"
    "s_bitcmp1_b32 s46, 4\n\t"
    "s_cselect_b32 exec_hi, -1, 0\n\t"
    "ds_write_b64 v196, v[244:245] offset:1280\n\t"
    "v_mov_b64 v[244:245], 0\n\t"
    "v_add_u32 v196, 0x100, v196\n\t"
    "s_mov_b64 exec, -1\n\t"
    "s_branch 24b\n\t"
    "21:\n\t"
    "s_bitcmp1_b32 s46, 1\n\t"
    "s_cselect_b32 exec_lo, -1, 0\n\t"
    "s_bitcmp1_b32 s46, 5\n\t"
    "s_cselect_b32 exec_hi, -1, 0\n\t"
    "ds_write_b64 v197, v[246:247] offset:1280\n\t"
    "v_mov_b64 v[246:247], 0\n\t"
    "v_add_u32 v197, 0x100, v197\n\t"
    "s_mov_b64 exec, -1\n\t"
    "s_branch 25b\n\t"
    "22:\n\t"
    "s_bitcmp1_b32 s46, 2\n\t"
    "s_cselect_b32 exec_lo, -1, 0\n\t"
    "s_bitcmp1_b32 s46, 6\n\t"
    "s_cselect_b32 exec_hi, -1, 0\n\t"
    "ds_write_b64 v198, v[248:249] offset:1280\n\t"
    "v_mov_b64 v[248:249], 0\n\t"
    "v_add_u32 v198, 0x100, v198\n\t"
    "s_mov_b64 exec, -1\n\t"
    "s_branch 26b\n\t"
    "23:\n\t"
    "s_bitcmp1_b32 s46, 3\n\t"
    "s_cselect_b32 exec_lo, -1, 0\n\t"
    "s_bitcmp1_b32 s46, 7\n\t"
    "s_cselect_b32 exec_hi, -1, 0\n\t"
    "ds_write_b64 v199, v[250:251] offset:1280\n\t"
    "v_mov_b64 v[250:251], 0\n\t"
    "v_add_u32 v199, 0x100, v199\n\t"
    "s_mov_b64 exec, -1\n\t"
    "s_branch 27b\n\t"
    "30:\n\t"
    "s_bitcmp1_b32 s44, 0\n\t"
    "s_cselect_b32 exec_lo, -1, 0\n\t"
    "s_bitcmp1_b32 s44, 4\n\t"
    "s_cselect_b32 exec_hi, -1, 0\n\t"
    "ds_write_b64 v196, v[244:245] offset:1280\n\t"
    "v_mov_b64 v[244:245], 0\n\t"
    "v_add_u32 v196, 0x100, v196\n\t"
    "s_mov_b64 exec, -1\n\t"
    "s_branch 34b\n\t"
    "31:\n\t"
    "s_bitcmp1_b32 s44, 1\n\t"
    "s_cselect_b32 exec_lo, -1, 0\n\t"
    "s_bitcmp1_b32 s44, 5\n\t"
    "s_cselect_b32 exec_hi, -1, 0\n\t"
    "ds_write_b64 v197, v[246:247] offset:1280\n\t"
    "v_mov_b64 v[246:247], 0\n\t"
    "v_add_u32 v197, 0x100, v197\n\t"
    "s_mov_b64 exec, -1\n\t"
    "s_branch 35b\n\t"
    "32:\n\t"
    "s_bitcmp1_b32 s44, 2\n\t"
    "s_cselect_b32 exec_lo, -1, 0\n\t"
    "s_bitcmp1_b32 s44, 6\n\t"
    "s_cselect_b32 exec_hi, -1, 0\n\t"
    "ds_write_b64 v198, v[248:249] offset:1280\n\t"
    "v_mov_b64 v[248:249], 0\n\t"
    "v_add_u32 v198, 0x100, v198\n\t"
    "s_mov_b64 exec, -1\n\t"
    "s_branch 36b\n\t"
    "33:\n\t"
    "s_bitcmp1_b32 s44, 3\n\t"
    "s_cselect_b32 exec_lo, -1, 0\n\t"
    "s_bitcmp1_b32 s44, 7\n\t"
    "s_cselect_b32 exec_hi, -1, 0\n\t"
    "ds_write_b64 v199, v[250:251] offset:1280\n\t"
    "v_mov_b64 v[250:251], 0\n\t"
    "v_add_u32 v199, 0x100, v199\n\t"
    "s_mov_b64 exec, -1\n\t"
    "s_branch 37b\n\t"
    "9:\n\t"
    "s_mov_b64 s[48:49], s[44:45]\n\t"
    "s_mov_b64 s[44:45], s[46:47]\n\t"
    "s_mov_b64 s[46:47], s[48:49]\n\t"
    "4:\n\t"
    "s_setprio 0\n\t"
    "s_and_b32 %[tmp], s44, 17\n\t"
    "s_cbranch_scc1 50f\n\t"
    "54:\n\t"
    "s_and_b32 %[tmp], s44, 34\n\t"
    "s_cbranch_scc1 51f\n\t"
    "55:\n\t"
    "s_and_b32 %[tmp], s44, 68\n\t"
    "s_cbranch_scc1 52f\n\t"
    "56:\n\t"
    "s_and_b32 %[tmp], s44, 136\n\t"
    "s_cbranch_scc1 53f\n\t"
    "57:\n\t"
    "v_rcp_f32 v132, v132\n\t"
    "v_rcp_f32 v133, v133\n\t"
    "v_rcp_f32 v140, v140\n\t"
    "v_rcp_f32 v141, v141\n\t"
    "v_rcp_f32 v136, v136\n\t"
    "v_rcp_f32 v137, v137\n\t"
    "v_rcp_f32 v144, v144\n\t"
    "v_rcp_f32 v145, v145\n\t"
    "v_fmac_f32 v244, v134, v132\n\t"
    "v_fmac_f32 v246, v135, v133\n\t"
    "v_fmac_f32 v248, v142, v140\n\t"
    "v_fmac_f32 v250, v143, v141\n\t"
    "v_fmac_f32 v245, v138, v136\n\t"
    "v_fmac_f32 v247, v139, v137\n\t"
    "v_fmac_f32 v249, v146, v144\n\t"
    "v_fmac_f32 v251, v147, v145\n\t"
    "s_waitcnt vmcnt(0)\n\t"
    "ds_write_b64 v243, v[244:245] offset:0\n\t"
    "ds_write_b64 v243, v[246:247] offset:1024\n\t"
    "ds_write_b64 v243, v[248:249] offset:2048\n\t"
    "ds_write_b64 v243, v[250:251] offset:3072\n\t"
    "s_bitcmp1_b32 s46, 0\n\t"
    "s_cselect_b32 exec_lo, -1, 0\n\t"
    "s_bitcmp1_b32 s46, 4\n\t"
    "s_cselect_b32 exec_hi, -1, 0\n\t"
    "ds_write_b64 v196, v[244:245] offset:1280\n\t"
    "s_bitcmp1_b32 s46, 1\n\t"
    "s_cselect_b32 exec_lo, -1, 0\n\t"
    "s_bitcmp1_b32 s46, 5\n\t"
    "s_cselect_b32 exec_hi, -1, 0\n\t"
    "ds_write_b64 v197, v[246:247] offset:1280\n\t"
    "s_bitcmp1_b32 s46, 2\n\t"
    "s_cselect_b32 exec_lo, -1, 0\n\t"
    "s_bitcmp1_b32 s46, 6\n\t"
    "s_cselect_b32 exec_hi, -1, 0\n\t"
    "ds_write_b64 v198, v[248:249] offset:1280\n\t"
    "s_bitcmp1_b32 s46, 3\n\t"
    "s_cselect_b32 exec_lo, -1, 0\n\t"
    "s_bitcmp1_b32 s46, 7\n\t"
    "s_cselect_b32 exec_hi, -1, 0\n\t"
    "ds_write_b64 v199, v[250:251] offset:1280\n\t"
    "s_mov_b64 exec, -1\n\t"
    "s_branch 8f\n\t"
    "50:\n\t"
    "s_bitcmp1_b32 s44, 0\n\t"
    "s_cselect_b32 exec_lo, -1, 0\n\t"
    "s_bitcmp1_b32 s44, 4\n\t"
    "s_cselect_b32 exec_hi, -1, 0\n\t"
    "ds_write_b64 v196, v[244:245] offset:1280\n\t"
    "v_mov_b64 v[244:245], 0\n\t"
    "v_add_u32 v196, 0x100, v196\n\t"
    "s_mov_b64 exec, -1\n\t"
    "s_branch 54b\n\t"
    "51:\n\t"
    "s_bitcmp1_b32 s44, 1\n\t"
    "s_cselect_b32 exec_lo, -1, 0\n\t"
    "s_bitcmp1_b32 s44, 5\n\t"
    "s_cselect_b32 exec_hi, -1, 0\n\t"
    "ds_write_b64 v197, v[246:247] offset:1280\n\t"
    "v_mov_b64 v[246:247], 0\n\t"
    "v_add_u32 v197, 0x100, v197\n\t"
    "s_mov_b64 exec, -1\n\t"
    "s_branch 55b\n\t"
    "52:\n\t"
    "s_bitcmp1_b32 s44, 2\n\t"
    "s_cselect_b32 exec_lo, -1, 0\n\t"
    "s_bitcmp1_b32 s44, 6\n\t"
    "s_cselect_b32 exec_hi, -1, 0\n\t"
    "ds_write_b64 v198, v[248:249] offset:1280\n\t"
    "v_mov_b64 v[248:249], 0\n\t"
    "v_add_u32 v198, 0x100, v198\n\t"
    "s_mov_b64 exec, -1\n\t"
    "s_branch 56b\n\t"
    "53:\n\t"
    "s_bitcmp1_b32 s44, 3\n\t"
    "s_cselect_b32 exec_lo, -1, 0\n\t"
    "s_bitcmp1_b32 s44, 7\n\t"
    "s_cselect_b32 exec_hi, -1, 0\n\t"
    "ds_write_b64 v199, v[250:251] offset:1280\n\t"
    "v_mov_b64 v[250:251], 0\n\t"
    "v_add_u32 v199, 0x100, v199\n\t"
    "s_mov_b64 exec, -1\n\t"
    "s_branch 57b\n\t"
    "8:\n\t"
            : "=&{v[244:251]}"(acc), "+{v[212:219]}"(pbr), "+{v[220:223]}"(aer), "+{v[224:231]}"(dpr), "+{v[232:239]}"(msc),
              "+{v[196:199]}"(ra), [i] "=&s"(si), [tmp] "=&s"(stmp)
            : "{v[20:51]}"(wg), "{v[52:83]}"(wvv), "{v[240:241]}"(msd), "{v243}"(ta), [T] "s"(T), [ring] "s"(ring), [srd0] "s"(srdi[0]),
              [srd1] "s"(srdi[1]), [nrec] "s"(NN), [ev0] "s"(ev0), [ev1] "s"(ev1)
            : "memory", "scc", "s40", "s41", "s42", "s43", "s44", "s45", "s46", "s47", "s48", "s49",
              "v84", "v85", "v86", "v87", "v88", "v89", "v90", "v91", "v92", "v93", "v94", "v95", "v96", "v97", "v98", "v99",
              "v100", "v101", "v102", "v103", "v104", "v105", "v106", "v107", "v108", "v109", "v110", "v111", "v112", "v113", "v114", "v115",
              "v116", "v117", "v118", "v119", "v132", "v133", "v134", "v135", "v136", "v137", "v138", "v139", "v140", "v141", "v142", "v143",
              "v144", "v145", "v146", "v147", "v148", "v149", "v150", "v151", "v152", "v153", "v154", "v155", "v156", "v157", "v158", "v159",
              "v160", "v161", "v162", "v163", "v164", "v165", "v166", "v167", "v168", "v169", "v170", "v171", "v172", "v173", "v174", "v175",
              "v176", "v177", "v178", "v179", "v180", "v181", "v182", "v183", "v184", "v185", "v186", "v187", "v188", "v189", "v190", "v191",
              "v192", "v193", "v194", "v195", "v242"
        );
    } else {
        BAR();
        asm volatile("s_waitcnt vmcnt(0)" ::: "memory");
    }
    const __amdgpu_buffer_rsrc_t rout = RSRC(out, min(b * NPB + NPB, NN) * 128);
    {
        unsigned ft[7];
#pragma unroll
        for (int i = 0; i < 7; ++i) ft[i] = LDS(unsigned, tpa + i * 4) + c8;
        const unsigned multi = LDS(unsigned, L_MULTI);
        asm volatile("" : "+v"(ft[0]), "+v"(ft[1]), "+v"(ft[2]), "+v"(ft[3]), "+v"(ft[4]), "+v"(ft[5]), "+v"(ft[6]));
        BAR();
        if (__builtin_expect(__builtin_amdgcn_readfirstlane(multi), 0)) {
            const unsigned r = min(wv * 14 + lane, NPB);
            const unsigned qs = (r * 272 - LDS(unsigned, L_QB + r * 4)) >> 4, tp = LDS(unsigned, L_TP + r * 4);
            const bool lo = qs < 32 * T1;
            const unsigned sf = (unsigned)(((float)(lo ? qs : qs - 32 * T1) + 0.5f) * __builtin_amdgcn_rcpf((float)(lo ? T1 : T2))) + (lo ? 0 : 32);
            const unsigned a0 = L_RING + sf * 1024;
#pragma unroll 1
            for (unsigned long long m = __ballot(lane < 14 && a0 < tp); m; m &= m - 1) {
                const int l = __builtin_ctzll(m);
                const unsigned e = __builtin_amdgcn_readlane(tp, l), row = L_AGG + (wv * 14 + l) * 256 + lane * 4;
                float acc = LDS(float, row);
#pragma unroll 1
                for (unsigned a = __builtin_amdgcn_readlane(a0, l); a < e; a += 1024) acc += LDS(float, a + lane * 4);
                LDS(float, row) = acc;
            }
        }
        f32x2 g[7], t[7];
#pragma unroll
        for (int i = 0; i < 7; ++i) {
            g[i] = LDS(f32x2, ga + i * 256);
            t[i] = LDS(f32x2, ft[i]);
        }
        __builtin_amdgcn_sched_barrier(0);
#pragma unroll
        for (int i = 0; i < 7; ++i) g[i] += t[i];
        __builtin_amdgcn_sched_barrier(0);
        f32x2 z[7];
#pragma unroll
        for (int i = 0; i < 7; ++i) z[i] = __builtin_elementwise_fma(g[i], f32x2{VUNSCALE * NLOG2E, VUNSCALE * NLOG2E}, uu[i]);
        __builtin_amdgcn_sched_barrier(0);
#pragma unroll
        for (int i = 0; i < 7; ++i) t[i] = f32x2{__builtin_amdgcn_exp2f(z[i][0]), __builtin_amdgcn_exp2f(z[i][1])};
        __builtin_amdgcn_sched_barrier(0);
#pragma unroll
        for (int i = 0; i < 7; ++i) t[i] = __builtin_elementwise_fma(t[i], f32x2{NLOG2E, NLOG2E}, f32x2{NLOG2E, NLOG2E});
        __builtin_amdgcn_sched_barrier(0);
#pragma unroll
        for (int i = 0; i < 7; ++i) t[i] = f32x2{__builtin_amdgcn_rcpf(t[i][0]), __builtin_amdgcn_rcpf(t[i][1])};
        __builtin_amdgcn_sched_barrier(0);
#pragma unroll
        for (int i = 0; i < 7; ++i) z[i] *= t[i];
        __builtin_amdgcn_sched_barrier(0);
#pragma unroll
        for (int i = 0; i < 7; ++i) LDS(unsigned, ob + i * 128) = pack_bf2(z[i][0], z[i][1]);
        __builtin_amdgcn_raw_buffer_store_b128(LDS(u32x4, os), rout, o16, 0, OAUX);
        __builtin_amdgcn_raw_buffer_store_b128(LDS(u32x4, os + 1024), rout, o17, 0, OAUX);
        __builtin_amdgcn_sched_barrier(0);
        f32x2 chk = ((g[0] + g[1]) + (g[2] + g[3])) + ((g[4] + g[5]) + g[6]);
        chk *= 0.0f;
        if (wv < 7 && chk[0] + chk[1] != 0.0f) slow = true;
    }
    if (w == 0 && tid == 0) __hip_atomic_store((gu64*)(ws + WS_EPOCH), next_epoch, RLX_AGENT);
    if (slow) LDS(unsigned, L_SLOW) = 1;
    BAR();
    if (LDS(unsigned, L_SLOW)) {
        float* ag = (float*)(lds + L_AGG);
        unsigned* wl = (unsigned*)(lds + L_RING);
#pragma unroll 1
        for (int k = tid; k < NPB * 64; k += 512) ag[k] = 0.f;
#pragma unroll 1
        for (int k = tid; k < 196 * 32; k += 512) wl[k] = k < 132 * 32 ? gw[k] : vw[k - 132 * 32];
        BAR();
        const unsigned short* w16 = (const unsigned short*)wl + lane;
        for (int p = wv * 32; p < wv * 32 + 32; ++p) {
            gu32* inf = (gu32*)(ws + WS_EV + (size_t)p * ECH);
            const unsigned i0 = __hip_atomic_load(inf + b, RLX_AGENT), es = (i0 & 0xffffu) >> 2, of = i0 >> 18;
            const unsigned en = b < NB - 1 ? (__hip_atomic_load(inf + b + 1, RLX_AGENT) & 0xffffu) >> 2 : (unsigned)(PMAIN + min(max(PREST - p * PX, 0), PX)) * 2;
            for (unsigned r = 0; es + r < min(en, 3136u); ++r) {
                const unsigned key = r < 15 ? __hip_atomic_load((gu32*)(ws + WS_KEY + (size_t)p * KCH) + b * 16 + ((r + 1 + ((b << 1) & 12)) & 15), RLX_AGENT)
                                            : __hip_atomic_load((gu32*)(ws + WS_OK + (size_t)p * KCH) + ((of + r - 15) & 4095u), RLX_AGENT);
                const unsigned long long ee = __hip_atomic_load((gu64*)(inf + 256) + es + r, RLX_AGENT);
                const unsigned dl = min((key >> 18) & 127u, (unsigned)NPB - 1), sn = min(key & 0x7fffu, (unsigned)NN - 1);
                const float xs = bf2f(x[sn * 64 + lane]), xd = bf2f(x[min(b * NPB + dl, (unsigned)NN - 1) * 64 + lane]);
                float z = 0.f, hv = 0.f;
#pragma unroll 4
                for (int k = 0; k < 64; ++k) {
                    const float a = __shfl(xd, k), g = __shfl(xs, k);
                    z = __builtin_fmaf(a, bf2f(w16[k * 64]), z);
                    z = __builtin_fmaf(g, bf2f(w16[(64 + k) * 64]), z);
                    hv = __builtin_fmaf(g, bf2f(w16[(132 + k) * 64]), hv);
                }
                for (int k = 0; k < 4; ++k) z = __builtin_fmaf(bf2f((unsigned short)(ee >> (16 * k))), bf2f(w16[(128 + k) * 64]), z);
                atomicAdd(&ag[dl * 64 + lane], hv * __builtin_amdgcn_rcpf(1.0f + __builtin_amdgcn_exp2f(z * NLOG2E)));
            }
        }
        BAR();
#pragma unroll
        for (int i = 0; i < 7; ++i) {
            const f32x2 z = LDS(f32x2, ga + i * 256) * NLOG2E + uu[i];
            const float r0 = z[0] * __builtin_amdgcn_rcpf(NLOG2E + NLOG2E * __builtin_amdgcn_exp2f(z[0]));
            const float r1 = z[1] * __builtin_amdgcn_rcpf(NLOG2E + NLOG2E * __builtin_amdgcn_exp2f(z[1]));
            __builtin_amdgcn_raw_buffer_store_b32(pack_bf2(r0, r1), rout, (unsigned)(b * NPB + wv * 14 + h * 7 + i) * 128 + c * 4, 0, 0);
        }
    }
}

extern "C" void kernel_launch(void* const* d_in, const int* in_sizes, int n_in, void* d_out, int out_size, void* d_ws,
                              size_t ws_size, hipStream_t stream) {
    if (n_in != 7 || in_sizes[0] != NN * 64 || in_sizes[2] != NE || out_size != NN * 64 || ws_size < WS_END) return;
    (void)hipFuncSetAttribute((const void*)k_egc, hipFuncAttributeMaxDynamicSharedMemorySize, L_TOTAL);
    k_egc<<<NB, 512, L_TOTAL, stream>>>((const unsigned short*)d_in[0], (const unsigned*)d_in[1], (const unsigned*)d_in[2],
                                        (const unsigned*)d_in[3], (const unsigned*)d_in[4], (const unsigned*)d_in[5],
                                        (const unsigned*)d_in[6], (unsigned char*)d_ws, (unsigned short*)d_out);
}
```
